```python
import torch

def _softmax(x, axis=-1):
    return torch.softmax(x, dim=axis)
def _gelu(x):
    return torch.nn.functional.gelu(x, approximate="tanh")
def _silu(x):
    return torch.nn.functional.silu(x)
def _sigmoid(x):
    return torch.sigmoid(x)
def _layernorm(x, axis=-1, eps=1e-5):
    m = x.mean(dim=axis, keepdim=True)
    v = x.var(dim=axis, keepdim=True, unbiased=False)
    return (x - m) / torch.sqrt(v + eps)
def _scatter_add(tgt, idx, src, axis=0):
    return tgt.clone().index_add_(axis, idx, src)
def _segment_sum(src, seg, n):
    out = torch.zeros((n,) + src.shape[1:], dtype=src.dtype)
    return out.index_add_(0, seg, src)
def _pad(x, pw):
    flat = tuple(v for pair in reversed(pw) for v in pair)
    return torch.nn.functional.pad(x, flat)

kernel_name = 'parallel_sum__linear_attn_block__geglu_ffn__prompt'

def setup_inputs():

    _w = (lambda a: (lambda t: t.to(torch.bfloat16) if t.is_floating_point() else t)(torch.as_tensor(a)))
    def _A_setup():
        B, S, D = 1, 2048, 640
        return {
            "x": _w((lambda *s: torch.randn(*s, dtype=torch.bfloat16) * 0.05)(B, S, D)),
            "wqkv": _w((lambda *s: torch.randn(*s, dtype=torch.bfloat16) * 0.2)(D, 3 * D)),
            "wo": _w((lambda *s: torch.randn(*s, dtype=torch.bfloat16) * 0.2)(D, D)),
        }
    _A_in = _A_setup()

    def _B_setup():
        B, S, D, F = 1, 2048, 640, 1728
        return {
            "x": _w((lambda *s: torch.randn(*s, dtype=torch.bfloat16) * 0.05)(B, S, D)),
            "w_gate": _w((lambda *s: torch.randn(*s, dtype=torch.bfloat16) * 0.05)(D, F)),
            "w_up": _w((lambda *s: torch.randn(*s, dtype=torch.bfloat16) * 0.05)(D, F)),
            "w_down": _w((lambda *s: torch.randn(*s, dtype=torch.bfloat16) * 0.05)(F, D)),
        }
    _B_in = _B_setup()

    return {
        "A_x": _A_in["x"],
        "A_wqkv": _A_in["wqkv"],
        "A_wo": _A_in["wo"],
        "B_x": _B_in["x"],
        "B_w_gate": _B_in["w_gate"],
        "B_w_up": _B_in["w_up"],
        "B_w_down": _B_in["w_down"],
    }

def reference(A_x, A_wqkv, A_wo, B_x, B_w_gate, B_w_up, B_w_down):
    def _A_ref(x, wqkv, wo):
        B, S, D = x.shape
        H = 32; Dh = D // H
        qkv = (lambda x, *d: x.permute(*d))((x @ wqkv).reshape(B, S, 3, H, Dh), 2, 0, 3, 1, 4)
        q, k, v = qkv[0], qkv[1], qkv[2]
        phi = lambda t: torch.where(t > 0, t + 1, torch.exp(t)) + 1e-6
        pq, pk = phi(q), phi(k)
        kv = torch.einsum("bhsd,bhse->bhde", pk, v)
        z = torch.einsum("bhsd->bhd", pk)
        num = torch.einsum("bhqd,bhde->bhqe", pq, kv)
        den = torch.einsum("bhqd,bhd->bhq", pq, z)[..., None] + 1e-6
        o = num / den
        return (lambda x, *d: x.permute(*d))(o, 0, 2, 1, 3).reshape(B, S, D) @ wo

    def _B_ref(x, w_gate, w_up, w_down):
        return (_gelu(x @ w_gate) * (x @ w_up)) @ w_down

    x = A_x
    xn = _layernorm(x, axis=-1)
    return x + _A_ref(xn, A_wqkv, A_wo) + _B_ref(xn, B_w_gate, B_w_up, B_w_down)
```

```cpp
#include <hip/hip_runtime.h>
#include <hip/hip_bf16.h>
typedef __hip_bfloat16 bf16;
static __device__ __forceinline__ float b2f(bf16 v){return __bfloat162float(v);}
static __device__ __forceinline__ bf16 f2b(float v){return __float2bfloat16(v);}
static __device__ __forceinline__ float rb(float v){return b2f(f2b(v));}
constexpr int S=2048,D=640,F=1728,H=32,DH=20;

__global__ void ln_k(const bf16* x, bf16* xn){
  int row=blockIdx.x; int t=threadIdx.x;
  float s=0; for(int i=t;i<D;i+=64) s+=b2f(x[row*D+i]);
  for(int o=32;o;o>>=1) s+=__shfl_xor(s,o);
  float m=s/D; float v=0; for(int i=t;i<D;i+=64){float d=b2f(x[row*D+i])-m; v+=d*d;}
  for(int o=32;o;o>>=1) v+=__shfl_xor(v,o);
  v/=D; float r=1.f/sqrtf(v+1e-5f);
  for(int i=t;i<D;i+=64) xn[row*D+i]=f2b((b2f(x[row*D+i])-m)*r);
}
__global__ void gemm_k(const bf16* A,const bf16* B,float* C,int M,int N,int K){
  __shared__ float As[16][17],Bs[16][17];
  int tx=threadIdx.x&15,ty=threadIdx.x>>4; int r=blockIdx.y*16+ty,c=blockIdx.x*16+tx; float acc=0;
  for(int k0=0;k0<K;k0+=16){ As[ty][tx]=b2f(A[r*K+k0+tx]); Bs[ty][tx]=b2f(B[(k0+ty)*N+c]); __syncthreads();
    for(int k=0;k<16;k++) acc+=As[ty][k]*Bs[k][tx]; __syncthreads(); }
  C[r*N+c]=acc;
}
__device__ float phi(float t){ t=rb(t); float r= t>0? rb(t+1.f): rb(expf(t)); return rb(r+1e-6f);}
__global__ void kv_k(const float* qkv,float* kv,float* z){
  int h=blockIdx.x; int t=threadIdx.x; if(t>=420) return; int d=t/21,e=t%21; float acc=0;
  for(int s=0;s<S;s++){ float pk=phi(qkv[s*1920+640+h*20+d]); float v= e<20? rb(qkv[s*1920+1280+h*20+e]):1.f; acc+=pk*v; }
  if(e<20) kv[h*400+d*20+e]=rb(acc); else z[h*20+d]=rb(acc);
}
__global__ void o_k(const float* qkv,const float* kv,const float* z,bf16* o){
  int s=blockIdx.x,t=threadIdx.x; int h=t/20,e=t%20; float num=0,den=0;
  for(int d=0;d<20;d++){ float pq=phi(qkv[s*1920+h*20+d]); num+=pq*kv[h*400+d*20+e]; den+=pq*z[h*20+d]; }
  o[s*640+t]=f2b(rb(num)/rb(rb(den)+1e-6f));
}
__global__ void h_k(const float* g,const float* u,bf16* h,int n){ int i=blockIdx.x*256+threadIdx.x; if(i>=n) return;
  float x=rb(g[i]); float y=0.5f*x*(1.f+tanhf(0.7978845608f*(x+0.044715f*x*x*x))); h[i]=f2b(rb(y)*rb(u[i])); }
__global__ void out_k(const bf16* x,const float* a,const float* b,bf16* out,int n){ int i=blockIdx.x*256+threadIdx.x; if(i>=n) return;
  out[i]=f2b(rb(b2f(x[i])+rb(a[i]))+rb(b[i])); }

extern "C" void kernel_launch(void* const* d_in, const int* in_sizes, int n_in, void* d_out, int out_size, void* d_ws, size_t ws_size, hipStream_t stream){
  const bf16* x=(const bf16*)d_in[0]; const bf16* wqkv=(const bf16*)d_in[1]; const bf16* wo=(const bf16*)d_in[2];
  const bf16* wg=(const bf16*)d_in[4]; const bf16* wu=(const bf16*)d_in[5]; const bf16* wd=(const bf16*)d_in[6];
  char* w=(char*)d_ws; bf16* xn=(bf16*)w; w+=S*D*2; float* qkv=(float*)w; w+=S*1920*4; float* g=(float*)w; w+=S*F*4; float* u=(float*)w; w+=S*F*4;
  float* kv=(float*)w; w+=H*400*4; float* z=(float*)w; w+=H*20*4; bf16* o=(bf16*)w; w+=S*D*2; bf16* hh=(bf16*)w; w+=S*F*2; float* a=(float*)w; w+=S*D*4; float* b=(float*)w;
  ln_k<<<S,64,0,stream>>>(x,xn);
  gemm_k<<<dim3(1920/16,S/16),256,0,stream>>>(xn,wqkv,qkv,S,1920,D);
  gemm_k<<<dim3(F/16,S/16),256,0,stream>>>(xn,wg,g,S,F,D);
  gemm_k<<<dim3(F/16,S/16),256,0,stream>>>(xn,wu,u,S,F,D);
  kv_k<<<H,448,0,stream>>>(qkv,kv,z);
  o_k<<<S,640,0,stream>>>(qkv,kv,z,o);
  h_k<<<(S*F+255)/256,256,0,stream>>>(g,u,hh,S*F);
  gemm_k<<<dim3(D/16,S/16),256,0,stream>>>(o,wo,a,S,D,D);
  gemm_k<<<dim3(D/16,S/16),256,0,stream>>>(hh,wd,b,S,D,F);
  out_k<<<(S*D+255)/256,256,0,stream>>>(x,a,b,(bf16*)d_out,S*D);
}
```

```cpp
#include <hip/hip_runtime.h>
#include <hip/hip_bf16.h>
#include <stdint.h>

typedef __attribute__((ext_vector_type(8))) __bf16 bf16x8;
typedef __attribute__((ext_vector_type(4))) __bf16 bf16x4;
typedef __attribute__((ext_vector_type(2))) __bf16 bf16x2;
typedef __attribute__((ext_vector_type(4))) short s16x4;
typedef __attribute__((ext_vector_type(4))) float f32x4;
typedef __attribute__((ext_vector_type(2))) float f32x2;
typedef __attribute__((ext_vector_type(4))) unsigned u32x4;
typedef __attribute__((ext_vector_type(2))) unsigned u32x2;
#define LAS __attribute__((address_space(3)))
#define GAS __attribute__((address_space(1)))
#define DEVI __device__ __forceinline__

constexpr int S_ = 2048, D_ = 640, F_ = 1728;
constexpr int NS2 = 74;
constexpr size_t XN_OFF = 0;
constexpr size_t AF_OFF = XN_OFF + (size_t)S_ * D_ * 2;
constexpr size_t WF_OFF = AF_OFF + (size_t)32 * NS2 * 4096;
constexpr size_t PQ_OFF = WF_OFF + (size_t)8 * NS2 * 5120;
constexpr size_t KVP_OFF = PQ_OFF + (size_t)128 * 160 * 128;
constexpr size_t STAMP_OFF = KVP_OFF + (size_t)8 * 32 * 420 * 4;

#define STAMP_AT(k, i) do {} while (0)

DEVI float bf_lo(unsigned v) { return __builtin_bit_cast(float, v << 16); }
DEVI float bf_hi(unsigned v) { return __builtin_bit_cast(float, v & 0xffff0000u); }
DEVI unsigned pack2(float a, float b) {
  f32x2 f = {a, b};
  return __builtin_bit_cast(unsigned, __builtin_convertvector(f, bf16x2));
}
DEVI __amdgpu_buffer_rsrc_t mk_rsrc(const void* p, unsigned bytes) {
  return __builtin_amdgcn_make_buffer_rsrc((void*)p, 0, bytes, 0x00020000);
}
DEVI f32x4 mfma16(bf16x8 a, bf16x8 b, f32x4 c) { return __builtin_amdgcn_mfma_f32_16x16x32_bf16(a, b, c, 0, 0, 0); }
DEVI s16x4 tr_read(const char* p) { return __builtin_amdgcn_ds_read_tr16_b64_v4i16((LAS s16x4*)p); }
DEVI bf16x8 cat8(s16x4 a, s16x4 b) {
  typedef __attribute__((ext_vector_type(8))) short s16x8;
  s16x8 r = __builtin_shufflevector(a, b, 0, 1, 2, 3, 4, 5, 6, 7);
  return __builtin_bit_cast(bf16x8, r);
}
DEVI float phi_f(float t) { return (t > 0.f ? t + 1.f : __expf(t)) + 1e-6f; }

__global__ __launch_bounds__(512) void k0_ln(const unsigned* __restrict__ x, char* __restrict__ ws) {
  STAMP_AT(0, 0);
  const int lane = threadIdx.x & 63, row = blockIdx.x * 8 + (threadIdx.x >> 6);
  const unsigned* xr = x + row * 320;
  unsigned v[5];
#pragma unroll
  for (int i = 0; i < 5; ++i) v[i] = xr[i * 64 + lane];
  float s = 0.f;
#pragma unroll
  for (int i = 0; i < 5; ++i) s += bf_lo(v[i]) + bf_hi(v[i]);
#pragma unroll
  for (int o = 32; o; o >>= 1) s += __shfl_xor(s, o);
  const float m = s * (1.f / 640.f);
  float q = 0.f;
#pragma unroll
  for (int i = 0; i < 5; ++i) { float a = bf_lo(v[i]) - m, b = bf_hi(v[i]) - m; q += a * a + b * b; }
#pragma unroll
  for (int o = 32; o; o >>= 1) q += __shfl_xor(q, o);
  const float r = rsqrtf(q * (1.f / 640.f) + 1e-5f);
  unsigned* o = (unsigned*)(ws + XN_OFF) + row * 320;
#pragma unroll
  for (int i = 0; i < 5; ++i) o[i * 64 + lane] = pack2((bf_lo(v[i]) - m) * r, (bf_hi(v[i]) - m) * r);
  STAMP_AT(0, 1);
}

constexpr int XB = 32768, WB = 24576, STG = XB + WB;
constexpr int K1_LDS = 2 * STG;

DEVI void dma16(__amdgpu_buffer_rsrc_t rs, char* lds, unsigned voff, unsigned soff) {
  __builtin_amdgcn_raw_ptr_buffer_load_lds(rs, (LAS void*)lds, 16, voff, soff, 0, 0);
}

template <int ROLE>
DEVI void k1_body(char* smem, const char* __restrict__ xn, const char* __restrict__ wA, const char* __restrict__ wB,
                  char* __restrict__ ws, int rt, int ct) {
  constexpr int NT = ROLE == 0 ? 6 : 5;
  constexpr int ROWB = ROLE == 0 ? 192 : 320;
  constexpr int WSTR = ROLE == 0 ? F_ * 2 : 1920 * 2;
  const int tid = threadIdx.x, lane = tid & 63;
  const int wid = __builtin_amdgcn_readfirstlane(tid >> 6);
  const int tg = wid & 3, ch = wid >> 2;
  const int fr = lane & 15, g = lane >> 4;

  const __amdgpu_buffer_rsrc_t rx = mk_rsrc(xn, S_ * D_ * 2);
  const __amdgpu_buffer_rsrc_t rw = mk_rsrc((ROLE == 0 && ch) ? wB : wA, D_ * WSTR);
  unsigned xo[4], wo_[3];
#pragma unroll
  for (int i = 0; i < 4; ++i) {
    int row = (wid * 4 + i) * 8 + (lane >> 3);
    int c = (lane & 7) ^ ((row >> 1) & 7);
    xo[i] = (unsigned)((rt * 256 + row) * 1280 + c * 16);
  }
  constexpr int CPR = ROWB / 16;
#pragma unroll
  for (int i = 0; i < 3; ++i) {
    int piece = ROLE == 0 ? (tg * 3 + i) : (i * 8 + wid);
    int ci = piece * 64 + lane;
    int r = ci / CPR, pc = ci % CPR;
    int lc = pc ^ (((r >> 3) & 1) << 1);
    int colb;
    if (ROLE == 0) colb = ct * 192 + lc * 16;
    else if (ROLE == 1) colb = ct * 320 + lc * 16;
    else colb = (lc < 10 ? 1280 + ct * 160 + lc * 16 : 2560 + ct * 160 + (lc - 10) * 16);
    wo_[i] = (unsigned)(r * WSTR + colb);
    if (ROLE != 0 && piece >= 20) wo_[i] = 0x7fffffffu;
  }
  auto stage = [&](int t, int b) {
    char* xb = smem + b * STG;
#pragma unroll
    for (int i = 0; i < 4; ++i) dma16(rx, xb + (wid * 4 + i) * 1024, xo[i], t * 128);
    char* wb = xb + XB;
#pragma unroll
    for (int i = 0; i < 3; ++i) {
      int piece = ROLE == 0 ? (wid * 3 + i) : (i * 8 + wid);
      dma16(rw, wb + piece * 1024, wo_[i], t * 64 * WSTR);
    }
  };
  unsigned xa[2];
#pragma unroll
  for (int kk = 0; kk < 2; ++kk) xa[kk] = (tg * 64 + fr) * 128 + (((kk * 4 + g) ^ (fr >> 1)) << 4);
  unsigned wa[NT];
  {
    const int q = (lane & 15) >> 2, p = lane & 3;
#pragma unroll
    for (int j = 0; j < NT; ++j) {
      int blk = 0, c;
      if (ROLE == 0) { blk = (j & 1) * 12288; c = (ch * 3 + (j >> 1)) * 32 + p * 8; }
      else if (ROLE == 1) c = (ch * 5 + j) * 32 + p * 8;
      else {
        if (j < 4) c = (j >> 1) * 160 + (ch * 2 + (j & 1)) * 40 + p * 8;
        else c = (p >> 1) * 160 + (ch * 2 + (p & 1)) * 40 + 32;
      }
      c ^= (g & 1) << 5;
      wa[j] = XB + blk + (g * 8 + q) * ROWB + c;
    }
  }
  f32x4 acc[4][NT];
#pragma unroll
  for (int i = 0; i < 4; ++i)
#pragma unroll
    for (int j = 0; j < NT; ++j) acc[i][j] = f32x4{0.f, 0.f, 0.f, 0.f};

  bf16x8 xf[2][4], wf[2][NT];
  auto rd = [&](int set, int b, int kk) {
    const char* base = smem + b * STG;
#pragma unroll
    for (int j = 0; j < NT; ++j) {
      const char* p = base + wa[j] + kk * 32 * ROWB;
      wf[set][j] = cat8(tr_read(p), tr_read(p + 4 * ROWB));
    }
#pragma unroll
    for (int i = 0; i < 4; ++i) xf[set][i] = *(const bf16x8*)(base + xa[kk] + i * 2048);
  };
  auto mm = [&](int set) {
#pragma unroll
    for (int i = 0; i < 4; ++i)
#pragma unroll
      for (int j = 0; j < NT; ++j)
        acc[i][j] = ROLE == 2 ? mfma16(xf[set][i], wf[set][j], acc[i][j]) : mfma16(wf[set][j], xf[set][i], acc[i][j]);
  };

  stage(0, 0);
  stage(1, 1);
  asm volatile("s_waitcnt vmcnt(7)" ::: "memory");
  __builtin_amdgcn_s_barrier();
  STAMP_AT(1, 1);
  rd(0, 0, 0);
#pragma unroll 2
  for (int t = 0; t < 10; ++t) {
    const int b = t & 1;
    rd(1, b, 1);
    mm(0);
    asm volatile("s_waitcnt vmcnt(0) lgkmcnt(0)" ::: "memory");
    __builtin_amdgcn_s_barrier();
    if (t + 2 < 10) stage(t + 2, b);
    if (t + 1 < 10) rd(0, b ^ 1, 0);
    mm(1);
  }
  STAMP_AT(1, 2);

  if (ROLE == 0) {
    char* af = ws + AF_OFF + ((size_t)(rt * 4 + tg) * NS2 + ct * 3) * 4096 + lane * 16;
#pragma unroll
    for (int i = 0; i < 4; ++i) {
      unsigned hp[3][2];
#pragma unroll
      for (int u = 0; u < 3; ++u) {
        float hv[4];
#pragma unroll
        for (int r = 0; r < 4; ++r) {
          float gv = acc[i][2 * u][r], uv = acc[i][2 * u + 1][r];
          const float c1 = -2.f * 0.7978845608f * 1.4426950409f, c2 = c1 * 0.044715f;
          float e = __builtin_amdgcn_exp2f(gv * (c1 + c2 * gv * gv));
          hv[r] = gv * uv * __builtin_amdgcn_rcpf(1.f + e);
        }
        hp[u][0] = pack2(hv[0], hv[1]);
        hp[u][1] = pack2(hv[2], hv[3]);
      }
      if (ch == 0) {
        *(u32x4*)(af + i * 1024) = u32x4{hp[0][0], hp[0][1], hp[1][0], hp[1][1]};
        *(u32x2*)(af + 4096 + i * 1024) = u32x2{hp[2][0], hp[2][1]};
      } else {
        *(u32x2*)(af + 4096 + i * 1024 + 8) = u32x2{hp[0][0], hp[0][1]};
        *(u32x4*)(af + 8192 + i * 1024) = u32x4{hp[1][0], hp[1][1], hp[2][0], hp[2][1]};
      }
    }
  } else if (ROLE == 1) {
#pragma unroll
    for (int i = 0; i < 4; ++i) {
      char* pq = ws + PQ_OFF + ((size_t)(rt * 16 + tg * 4 + i) * 160 + ct * 40 + ch * 20) * 128 + lane * 8;
#pragma unroll
      for (int j = 0; j < 5; ++j) {
        f32x4 a = acc[i][j];
        *(u32x2*)(pq + j * 512) = u32x2{pack2(phi_f(a[0]), phi_f(a[1])), pack2(phi_f(a[2]), phi_f(a[3]))};
      }
    }
  } else {
    bf16x8 op[2][5];
#pragma unroll
    for (int hh = 0; hh < 2; ++hh)
#pragma unroll
      for (int j = 0; j < 5; ++j) {
        f32x4 a = acc[2 * hh][j], b = acc[2 * hh + 1][j];
        const bool isk = j < 2 || (j == 4 && fr < 8);
        float v[8];
#pragma unroll
        for (int r = 0; r < 4; ++r) { v[r] = a[r]; v[4 + r] = b[r]; }
        if (j < 2 || j == 4) {
#pragma unroll
          for (int r = 0; r < 8; ++r) { float ph = phi_f(v[r]); v[r] = isk ? ph : v[r]; }
        }
        u32x4 pk = {pack2(v[0], v[1]), pack2(v[2], v[3]), pack2(v[4], v[5]), pack2(v[6], v[7])};
        op[hh][j] = __builtin_bit_cast(bf16x8, pk);
      }
    const u32x4 onesu = {0x3f803f80u, 0x3f803f80u, 0x3f803f80u, 0x3f803f80u};
    const bf16x8 ones = __builtin_bit_cast(bf16x8, onesu);
    f32x4 z[10];
#pragma unroll
    for (int n = 0; n < 10; ++n) z[n] = f32x4{0.f, 0.f, 0.f, 0.f};
#pragma unroll
    for (int hh = 0; hh < 2; ++hh) {
      z[0] = mfma16(op[hh][0], op[hh][2], z[0]);
      z[1] = mfma16(op[hh][1], op[hh][3], z[1]);
      z[2] = mfma16(op[hh][0], op[hh][4], z[2]);
      z[3] = mfma16(op[hh][1], op[hh][4], z[3]);
      z[4] = mfma16(op[hh][4], op[hh][2], z[4]);
      z[5] = mfma16(op[hh][4], op[hh][3], z[5]);
      z[6] = mfma16(op[hh][4], op[hh][4], z[6]);
      z[7] = mfma16(op[hh][0], ones, z[7]);
      z[8] = mfma16(op[hh][1], ones, z[8]);
      z[9] = mfma16(op[hh][4], ones, z[9]);
    }
    __syncthreads();
    float* red = (float*)smem + tg * 1680;
    float* hA = red + (ch * 2) * 420;
    float* hB = hA + 420;
    *(f32x4*)(hA + fr * 20 + g * 4) = z[0];
    *(f32x4*)(hB + fr * 20 + g * 4) = z[1];
    if (fr >= 8 && fr < 12) *(f32x4*)(hA + (fr + 8) * 20 + g * 4) = z[2];
    if (fr >= 12) *(f32x4*)(hB + (fr + 4) * 20 + g * 4) = z[3];
    if (g == 0) *(f32x4*)(hA + fr * 20 + 16) = z[4];
    if (g == 1) *(f32x4*)(hB + fr * 20 + 16) = z[5];
    if (g == 0 && fr >= 8 && fr < 12) *(f32x4*)(hA + (fr + 8) * 20 + 16) = z[6];
    if (g == 1 && fr >= 12) *(f32x4*)(hB + (fr + 4) * 20 + 16) = z[6];
    if (fr == 0) { *(f32x4*)(hA + 400 + g * 4) = z[7]; *(f32x4*)(hB + 400 + g * 4) = z[8]; }
    if (fr == 0 && g == 0) *(f32x4*)(hA + 416) = z[9];
    if (fr == 0 && g == 1) *(f32x4*)(hB + 416) = z[9];
    __syncthreads();
    if (tid < 420) {
      const f32x4* r4 = (const f32x4*)smem;
      f32x4 s = (r4[tid] + r4[420 + tid]) + (r4[840 + tid] + r4[1260 + tid]);
      *(f32x4*)(ws + KVP_OFF + ((size_t)(rt * 32 + ct * 4) * 420) * 4 + tid * 16) = s;
    }
  }
}

DEVI void k1_relayout(char* smem, const char* __restrict__ wd, const char* __restrict__ wo, char* __restrict__ ws, int id) {
  const int tid = threadIdx.x, lane = tid & 63;
  const int wid = __builtin_amdgcn_readfirstlane(tid >> 6);
  const int g = lane >> 4, q = (lane & 15) >> 2, p = lane & 3;
  for (int s0 = id; s0 < NS2; s0 += 32) {
#pragma unroll
    for (int u = 0; u < 2; ++u) {
      int s = s0 + u * 16;
      if (s < NS2) {
        const char* src = s < 54 ? wd + (size_t)s * 32 * 1280 : wo + (size_t)(s - 54) * 32 * 1280;
        const __amdgpu_buffer_rsrc_t rs = mk_rsrc(src, 40960);
#pragma unroll
        for (int i = 0; i < 5; ++i) dma16(rs, smem + u * 40960 + (wid * 5 + i) * 1024, (wid * 5 + i) * 1024 + lane * 16, 0);
      }
    }
    asm volatile("s_waitcnt vmcnt(0)" ::: "memory");
    __syncthreads();
#pragma unroll
    for (int u = 0; u < 2; ++u) {
      int s = s0 + u * 16;
      if (s < NS2) {
#pragma unroll
        for (int j = 0; j < 5; ++j) {
          const char* pp = smem + u * 40960 + (g * 4 + q) * 1280 + (wid * 5 + j) * 32 + p * 8;
          bf16x8 v = cat8(tr_read(pp), tr_read(pp + 16 * 1280));
          *(bf16x8*)(ws + WF_OFF + ((size_t)(wid * NS2 + s) * 5 + j) * 1024 + lane * 16) = v;
        }
      }
    }
    __syncthreads();
  }
}

__global__ __launch_bounds__(512, 2) void k1_gemm(const char* __restrict__ wqkv, const char* __restrict__ wo,
                                                  const char* __restrict__ wg, const char* __restrict__ wu,
                                                  const char* __restrict__ wd, char* __restrict__ ws) {
  extern __shared__ __attribute__((aligned(16))) char smem[];
  STAMP_AT(1, 0);
  const int bid = blockIdx.x, xcd = bid & 7, idx = bid >> 3;
  const int rt = (xcd & 1) * 4 + (idx & 3);
  const int cs = idx >> 2;
  const int ct = (((cs >> 1) * 4 + (xcd >> 1)) << 1) + (cs & 1);
  const char* xn = ws + XN_OFF;
  if (ct < 18) k1_body<0>(smem, xn, wg, wu, ws, rt, ct);
  else if (ct < 22) k1_body<1>(smem, xn, wqkv, wqkv, ws, rt, ct - 18);
  else if (ct < 30) k1_body<2>(smem, xn, wqkv, wqkv, ws, rt, ct - 22);
  else k1_relayout(smem, wd, wo, ws, (ct - 30) * 8 + rt);
  STAMP_AT(1, 3);
}

DEVI void o_compute(char* __restrict__ ws, int r, int head, int lane) {
  const int fr = lane & 15, g = lane >> 4;
  const float* kvp = (const float*)(ws + KVP_OFF) + head * 420;
  f32x4 a0 = {0, 0, 0, 0}, a0x = a0, a1 = a0, a1x = a0, a2 = a0, a2x = a0;
  const int e1 = 16 + (fr & 3);
#pragma unroll
  for (int p = 0; p < 8; ++p) {
    const float* b = kvp + p * 32 * 420;
    a0 += *(const f32x4*)(b + fr * 20 + g * 4);
    a0x += *(const f32x4*)(b + fr * 20 + 16);
    a1 += *(const f32x4*)(b + e1 * 20 + g * 4);
    a1x += *(const f32x4*)(b + e1 * 20 + 16);
    a2 += *(const f32x4*)(b + 400 + g * 4);
    a2x += *(const f32x4*)(b + 416);
  }
  auto mk = [&](f32x4 lo, f32x4 hi) {
    u32x4 v = {pack2(lo[0], lo[1]), pack2(lo[2], lo[3]), g == 0 ? pack2(hi[0], hi[1]) : 0u, g == 0 ? pack2(hi[2], hi[3]) : 0u};
    return __builtin_bit_cast(bf16x8, v);
  };
  const bf16x8 A0 = mk(a0, a0x), A1 = mk(a1, a1x), A2 = mk(a2, a2x);
#pragma unroll
  for (int i = 0; i < 4; ++i) {
    const char* pq = ws + PQ_OFF + ((size_t)(r * 4 + i) * 160 + head * 5) * 128;
    u32x2 lo = *(const u32x2*)(pq + lane * 8);
    u32x2 hi = *(const u32x2*)(pq + 512 + fr * 8);
    if (g != 0) hi = u32x2{0u, 0u};
    const bf16x8 B = __builtin_bit_cast(bf16x8, u32x4{lo[0], lo[1], hi[0], hi[1]});
    const f32x4 zz = {0, 0, 0, 0};
    f32x4 n0 = mfma16(A0, B, zz), n1 = mfma16(A1, B, zz), dn = mfma16(A2, B, zz);
    const float inv = 1.f / (dn[0] + 1e-6f);
    char* af = ws + AF_OFF + ((size_t)r * NS2 + 54) * 4096 + i * 1024 + fr * 16;
    {
      int Q = head * 5 + g;
      *(u32x2*)(af + (Q >> 3) * 4096 + (Q & 3) * 256 + ((Q >> 2) & 1) * 8) = u32x2{pack2(n0[0] * inv, n0[1] * inv), pack2(n0[2] * inv, n0[3] * inv)};
    }
    if (g == 0) {
      int Q = head * 5 + 4;
      *(u32x2*)(af + (Q >> 3) * 4096 + (Q & 3) * 256 + ((Q >> 2) & 1) * 8) = u32x2{pack2(n1[0] * inv, n1[1] * inv), pack2(n1[2] * inv, n1[3] * inv)};
    }
  }
}

DEVI void k3_map(int bid, int& r, int& c) {
  const int xcd = bid & 7, idx = bid >> 3;
  r = (xcd >> 1) * 8 + (idx & 7);
  c = (xcd & 1) * 4 + (idx >> 3);
}

__global__ __launch_bounds__(256) void k2_o(char* __restrict__ ws) {
  STAMP_AT(2, 0);
  int r, c;
  k3_map(blockIdx.x, r, c);
  o_compute(ws, r, c * 4 + (threadIdx.x >> 6), threadIdx.x & 63);
  STAMP_AT(2, 1);
}

constexpr int PD = 5;
__global__ __launch_bounds__(256, 1) void k3_gemm(const char* __restrict__ x, char* __restrict__ out, char* __restrict__ ws) {
  extern __shared__ __attribute__((aligned(16))) char smem[];
  STAMP_AT(3, 0);
  int r, c;
  k3_map(blockIdx.x, r, c);
  const int tid = threadIdx.x, lane = tid & 63;
  const int wid = __builtin_amdgcn_readfirstlane(tid >> 6);
  const int fr = lane & 15, g = lane >> 4;
  const char* ap = ws + AF_OFF + ((size_t)r * NS2 + wid) * 4096 + lane * 16;
  const char* wp = ws + WF_OFF + ((size_t)c * NS2 + wid) * 5120 + lane * 16;
  bf16x8 af[PD][4], wf[PD][5];
  f32x4 acc[4][5];
#pragma unroll
  for (int i = 0; i < 4; ++i)
#pragma unroll
    for (int j = 0; j < 5; ++j) acc[i][j] = f32x4{0.f, 0.f, 0.f, 0.f};
  auto ld = [&](int n) {
    const int slot = n % PD;
    const size_t so = (n == 18 && wid >= 2) ? (size_t)17 * 4 : (size_t)n * 4;
#pragma unroll
    for (int i = 0; i < 4; ++i) af[slot][i] = *(const bf16x8*)(ap + so * 4096 + i * 1024);
#pragma unroll
    for (int j = 0; j < 5; ++j) wf[slot][j] = *(const bf16x8*)(wp + so * 5120 + j * 1024);
  };
  const size_t obase = (size_t)(r * 64 + wid * 16 + fr) * 1280 + c * 160 + g * 8;
  u32x2 xr[5];
#pragma unroll
  for (int n = 0; n < PD; ++n) ld(n);
#pragma unroll
  for (int j = 0; j < 5; ++j) xr[j] = *(const u32x2*)(x + obase + j * 32);
  __builtin_amdgcn_sched_barrier(0);
#pragma unroll
  for (int n = 0; n < 19; ++n) {
    const int slot = n % PD;
    if (n < 18 || wid < 2) {
#pragma unroll
      for (int i = 0; i < 4; ++i)
#pragma unroll
        for (int j = 0; j < 5; ++j) acc[i][j] = mfma16(wf[slot][j], af[slot][i], acc[i][j]);
    }
    __builtin_amdgcn_sched_barrier(0);
    if (n + PD < 19) ld(n + PD);
    __builtin_amdgcn_sched_barrier(0);
  }
  STAMP_AT(3, 1);
  f32x4* red = (f32x4*)smem;
#pragma unroll
  for (int i = 0; i < 4; ++i) {
    if (i != wid) {
      const int sidx = wid < i ? wid : wid - 1;
#pragma unroll
      for (int j = 0; j < 5; ++j) red[((i * 3 + sidx) * 5 + j) * 64 + lane] = acc[i][j];
    }
  }
  __syncthreads();
  f32x4 fin[5];
#pragma unroll
  for (int j = 0; j < 5; ++j) fin[j] = wid == 0 ? acc[0][j] : wid == 1 ? acc[1][j] : wid == 2 ? acc[2][j] : acc[3][j];
#pragma unroll
  for (int sI = 0; sI < 3; ++sI)
#pragma unroll
    for (int j = 0; j < 5; ++j) fin[j] += red[((wid * 3 + sI) * 5 + j) * 64 + lane];
#pragma unroll
  for (int j = 0; j < 5; ++j) {
    u32x2 o = {pack2(fin[j][0] + bf_lo(xr[j][0]), fin[j][1] + bf_hi(xr[j][0])), pack2(fin[j][2] + bf_lo(xr[j][1]), fin[j][3] + bf_hi(xr[j][1]))};
    *(u32x2*)(out + obase + j * 32) = o;
  }
  STAMP_AT(3, 2);
}

extern "C" void kernel_launch(void* const* d_in, const int* in_sizes, int n_in, void* d_out, int out_size, void* d_ws,
                              size_t ws_size, hipStream_t stream) {
  const char* x = (const char*)d_in[0];
  const char* wqkv = (const char*)d_in[1];
  const char* wo = (const char*)d_in[2];
  const char* wg = (const char*)d_in[4];
  const char* wu = (const char*)d_in[5];
  const char* wd = (const char*)d_in[6];
  char* ws = (char*)d_ws;
  (void)hipFuncSetAttribute((const void*)k1_gemm, hipFuncAttributeMaxDynamicSharedMemorySize, K1_LDS);
  k0_ln<<<256, 512, 0, stream>>>((const unsigned*)x, ws);
  k1_gemm<<<256, 512, K1_LDS, stream>>>(wqkv, wo, wg, wu, wd, ws);
  k2_o<<<256, 256, 0, stream>>>(ws);
  k3_gemm<<<256, 256, 61440, stream>>>(x, (char*)d_out, ws);
}
```

```cpp
#include <hip/hip_runtime.h>
#include <hip/hip_bf16.h>
#include <stdint.h>

typedef __attribute__((ext_vector_type(8))) __bf16 bf16x8;
typedef __attribute__((ext_vector_type(2))) __bf16 bf16x2;
typedef __attribute__((ext_vector_type(4))) short s16x4;
typedef __attribute__((ext_vector_type(4))) float f32x4;
typedef __attribute__((ext_vector_type(2))) float f32x2;
typedef __attribute__((ext_vector_type(4))) unsigned u32x4;
typedef __attribute__((ext_vector_type(2))) unsigned u32x2;
typedef unsigned long long u64;
#define LAS __attribute__((address_space(3)))
#define GAS __attribute__((address_space(1)))
#define DEVI __device__ __forceinline__
#define RLX_AGENT __ATOMIC_RELAXED, __HIP_MEMORY_SCOPE_AGENT
typedef __amdgpu_buffer_rsrc_t rsrc_t;

constexpr int S_ = 2048, D_ = 640, F_ = 1728;
constexpr int NS2 = 74;
constexpr unsigned GEN_OFF = 0;
constexpr unsigned ALL_OFF = 4096;
constexpr unsigned BOX_OFF = 8192;
constexpr unsigned XN_OFF = BOX_OFF + 256 * 1024;
constexpr unsigned AF_OFF = XN_OFF + S_ * D_ * 2;
constexpr unsigned WF_OFF = AF_OFF + 32 * NS2 * 4096;
constexpr unsigned PQ_OFF = WF_OFF + 8 * NS2 * 5120;
constexpr unsigned KVP_OFF = PQ_OFF + 64 * 160 * 256;
constexpr unsigned WS_END = KVP_OFF + 8 * 32 * 420 * 4;
constexpr unsigned STAMP_OFF = (WS_END + 4095) & ~4095u;
constexpr int SL_XN = 0, SL_H = 32, SL_O = 50, SL_Q = 64, SL_KV = 65, SL_RL = 80;

#define STAMP_AT(k, i) do {} while (0)

DEVI float bf_lo(unsigned v) { return __builtin_bit_cast(float, v << 16); }
DEVI float bf_hi(unsigned v) { return __builtin_bit_cast(float, v & 0xffff0000u); }
DEVI unsigned pack2(float a, float b) {
  f32x2 f = {a, b};
  return __builtin_bit_cast(unsigned, __builtin_convertvector(f, bf16x2));
}
DEVI rsrc_t mk_rsrc(const void* p, unsigned bytes) { return __builtin_amdgcn_make_buffer_rsrc((void*)p, 0, bytes, 0x00020000); }
DEVI f32x4 mfma16(bf16x8 a, bf16x8 b, f32x4 c) { return __builtin_amdgcn_mfma_f32_16x16x32_bf16(a, b, c, 0, 0, 0); }
DEVI s16x4 tr_read(const char* p) { return __builtin_amdgcn_ds_read_tr16_b64_v4i16((LAS s16x4*)p); }
DEVI bf16x8 cat8(s16x4 a, s16x4 b) {
  typedef __attribute__((ext_vector_type(8))) short s16x8;
  s16x8 r = __builtin_shufflevector(a, b, 0, 1, 2, 3, 4, 5, 6, 7);
  return __builtin_bit_cast(bf16x8, r);
}
DEVI float phi_f(float t) { return (t > 0.f ? t + 1.f : __builtin_amdgcn_exp2f(t * 1.4426950409f)) + 1e-6f; }
template <int AUX> DEVI void dma16(rsrc_t rs, char* lds, unsigned voff, unsigned soff) {
  __builtin_amdgcn_raw_ptr_buffer_load_lds(rs, (LAS void*)lds, 16, voff, soff, 0, AUX);
}
DEVI void st16(rsrc_t rs, unsigned voff, unsigned soff, u32x4 v) { __builtin_amdgcn_raw_buffer_store_b128(v, rs, voff + soff, 0, 16); }
DEVI void st8(rsrc_t rs, unsigned voff, unsigned soff, u32x2 v) { __builtin_amdgcn_raw_buffer_store_b64(v, rs, voff, soff, 16); }
DEVI u32x4 ld16(rsrc_t rs, unsigned voff, unsigned soff) { return __builtin_amdgcn_raw_buffer_load_b128(rs, voff, soff, 16); }
DEVI void drain() { asm volatile("s_waitcnt vmcnt(0)" ::: "memory"); }
DEVI void bar() { __builtin_amdgcn_sched_barrier(0); asm volatile("s_barrier" ::: "memory"); __builtin_amdgcn_sched_barrier(0); }
DEVI void post(char* ws, int consumer, int slot, u64 tag) {
  __hip_atomic_store((GAS u64*)(ws + BOX_OFF + consumer * 1024 + slot * 8), tag, RLX_AGENT);
}
DEVI void await(const char* p, int n, u64 tag, int lane) {
  const GAS u64* q = (const GAS u64*)(p) + (lane < n ? lane : 0);
#pragma unroll 1
  for (int spin = 0; spin < (1 << 18); ++spin) {
    u64 v = __hip_atomic_load(q, RLX_AGENT);
    if (__all(v == tag)) break;
    __builtin_amdgcn_s_sleep(1);
  }
  __builtin_amdgcn_fence(__ATOMIC_ACQUIRE, "wavefront");
}
DEVI int lane_id() { int l; asm volatile("v_mbcnt_lo_u32_b32 %0, -1, 0\n\tv_mbcnt_hi_u32_b32 %0, -1, %0" : "=v"(l)); return l; }
DEVI bool arrive_last(char* cnt, unsigned n) {
  drain();
  unsigned old = 0;
  if (lane_id() == 0) old = __hip_atomic_fetch_add((LAS unsigned*)cnt, 1u, __ATOMIC_RELAXED, __HIP_MEMORY_SCOPE_WORKGROUP);
  return __builtin_amdgcn_readfirstlane(old) == n - 1;
}
DEVI u64 uni64(u64 v) { return ((u64)(unsigned)__builtin_amdgcn_readfirstlane((unsigned)(v >> 32)) << 32) | (unsigned)__builtin_amdgcn_readfirstlane((unsigned)v); }
DEVI int bid1(int rt, int ct) { const int m = ct >> 1; return (((((m >> 2) << 1) | (ct & 1)) * 4 + (rt & 3)) << 3) | ((m & 3) << 1) | (rt >> 2); }
DEVI int bid2(int r, int c) { return ((c * 4 + (r & 3)) << 3) | (r >> 2); }

constexpr int XB = 32768, WB = 24576;
constexpr int WBASE = 3 * XB, MISC = 3 * XB + 2 * WB;
constexpr int OSCR = MISC + 256;
constexpr int CNT = OSCR + 4 * 1792;
constexpr int LDS_BYTES = CNT + 16;

template <int ROLE>
DEVI void g1_loader(char* smem, rsrc_t rws, const char* __restrict__ wA, const char* __restrict__ wB, char* ws, int rt, int ct,
                    int lw, int lane) {
  constexpr int ROWB = ROLE == 0 ? 192 : 320;
  constexpr int WSTR = ROLE == 0 ? F_ * 2 : 1920 * 2;
  constexpr int NW = ROLE == 0 ? 6 : 5;
  constexpr int CPR = ROWB / 16;
  const rsrc_t rw = mk_rsrc((ROLE == 0 && lw >= 2) ? wB : wA, D_ * WSTR);
  unsigned wo_[NW];
#pragma unroll
  for (int i = 0; i < NW; ++i) {
    int piece = ROLE == 0 ? ((lw & 1) * 6 + i) : (lw * 5 + i);
    int ci = piece * 64 + lane;
    int r = ci / CPR, pc = ci % CPR;
    int lc = pc ^ (((r >> 3) & 1) << 1);
    int colb;
    if (ROLE == 0) colb = ct * 192 + lc * 16;
    else if (ROLE == 1) colb = ct * 320 + lc * 16;
    else colb = (lc < 10 ? 1280 + ct * 160 + lc * 16 : 2560 + ct * 160 + (lc - 10) * 16);
    wo_[i] = (unsigned)(r * WSTR + colb);
  }
  unsigned to;
  if (ROLE == 0) to = (unsigned)(((lane >> 5) * 64 + (rt & 3) * 16 + ((lane >> 1) & 15)) * WSTR + ct * 192 + (lane & 1) * 128);
  else {
    const int r = (rt & 3) * 16 + (lane >> 2), q = lane & 3;
    if (ROLE == 1) to = (unsigned)(r * WSTR + ct * 320 + (q < 3 ? q * 128 : 316));
    else to = (unsigned)(r * WSTR + 1280 * (1 + (q >> 1)) + ct * 160 + (q & 1) * 128);
  }
  auto touch = [&](int t) { __builtin_amdgcn_raw_ptr_buffer_load_lds(rw, (LAS void*)(smem + MISC), 4, to, t * 64 * WSTR, 0, 0); };
  auto stage_x = [&](int t, unsigned xb) {
#pragma unroll
    for (int n = 0; n < 8; ++n) dma16<16>(rws, smem + xb + (lw * 8 + n) * 1024, lane * 16, XN_OFF + (rt * 10 + t) * 32768 + (lw * 8 + n) * 1024);
  };
  auto stage_w = [&](int t, int b) {
#pragma unroll
    for (int n = 0; n < NW; ++n) dma16<0>(rw, smem + WBASE + b * WB + (lw * NW + n) * 1024, wo_[n], t * 64 * WSTR);
  };
  bar();
  if (ROLE == 0) touch((lw & 1) * 2); else touch(lw);
  bar();
  if (ROLE == 0) { touch(4 + (lw & 1) * 2); if (!(lw & 1)) touch(8); }
  else { touch(4 + lw); if (lw < 2) touch(8 + lw); }
  stage_w(0, 0);
  stage_w(1, 1);
  STAMP_AT(0, 1);
  const u64 tag = uni64(*(const u64*)(smem + CNT + 8));
  await(ws + BOX_OFF + blockIdx.x * 1024 + SL_XN * 8, 32, tag, lane);
  STAMP_AT(0, 2);
  stage_x(0, 0); stage_x(1, XB); stage_x(2, 2 * XB);
  asm volatile("s_waitcnt vmcnt(16)" ::: "memory");
  bar();
  unsigned xb = 0;
#pragma unroll 1
  for (int t = 0; t < 8; ++t) {
    asm volatile("s_waitcnt vmcnt(8)" ::: "memory");
    bar();
    stage_w(t + 2, t & 1);
    if (t + 3 < 10) stage_x(t + 3, xb);
    xb = xb == 2 * XB ? 0 : xb + XB;
  }
}

template <int ROLE>
DEVI void g1_compute(char* smem, rsrc_t rws, char* ws, int rt, int ct, int wid, int lane, u64 tag) {
  constexpr int NT = ROLE == 0 ? 6 : 5;
  constexpr int ROWB = ROLE == 0 ? 192 : 320;
  const int tg = wid & 3, ch = wid >> 2;
  const int fr = lane & 15, g = lane >> 4;
  unsigned xa[2];
#pragma unroll
  for (int kk = 0; kk < 2; ++kk) xa[kk] = (tg * 64 + fr) * 128 + (((kk * 4 + g) ^ (fr >> 1)) << 4);
  unsigned wa[NT];
  {
    const int q = (lane & 15) >> 2, p = lane & 3;
#pragma unroll
    for (int j = 0; j < NT; ++j) {
      int blk = 0, c;
      if (ROLE == 0) { blk = (j & 1) * 12288; c = (ch * 3 + (j >> 1)) * 32 + p * 8; }
      else if (ROLE == 1) c = (ch * 5 + j) * 32 + p * 8;
      else {
        if (j < 4) c = (j >> 1) * 160 + (ch * 2 + (j & 1)) * 40 + p * 8;
        else c = (p >> 1) * 160 + (ch * 2 + (p & 1)) * 40 + 32;
      }
      c ^= (g & 1) << 5;
      wa[j] = WBASE + blk + (g * 8 + q) * ROWB + c;
    }
  }
  f32x4 acc[4][NT];
#pragma unroll
  for (int i = 0; i < 4; ++i)
#pragma unroll
    for (int j = 0; j < NT; ++j) acc[i][j] = f32x4{0.f, 0.f, 0.f, 0.f};
  bf16x8 xf[2][4], wf[NT];
  auto rdw = [&](int j, int b, int kk) {
    const char* p = smem + b * WB + wa[j] + kk * 32 * ROWB;
    wf[j] = cat8(tr_read(p), tr_read(p + 4 * ROWB));
  };
  auto rdx = [&](int set, unsigned xaddr, int i) { xf[set][i] = *(const bf16x8*)(smem + xaddr + i * 2048); };
  auto mmj = [&](int set, int j) {
#pragma unroll
    for (int i = 0; i < 4; ++i)
      acc[i][j] = ROLE == 2 ? mfma16(xf[set][i], wf[j], acc[i][j]) : mfma16(wf[j], xf[set][i], acc[i][j]);
  };
  bar();
  STAMP_AT(1, 2);
#pragma unroll
  for (int i = 0; i < 4; ++i) rdx(0, xa[0], i);
#pragma unroll
  for (int j = 0; j < NT; ++j) rdw(j, 0, 0);
  __builtin_amdgcn_sched_barrier(0);
  unsigned xcur = 0;
  auto half = [&](int b) {
    const unsigned xnext = xcur == 2 * XB ? 0 : xcur + XB;
    const unsigned a1 = xa[1] + xcur, a0 = xa[0] + xnext;
#pragma unroll
    for (int j = 0; j < NT; ++j) {
      mmj(0, j);
      __builtin_amdgcn_sched_barrier(0);
      rdw(j, b, 1);
      if (j < 4) rdx(1, a1, j);
      __builtin_amdgcn_sched_barrier(0);
    }
    mmj(1, 0);
    __builtin_amdgcn_sched_barrier(0);
    asm volatile("s_waitcnt lgkmcnt(0)" ::: "memory");
    bar();
    __builtin_amdgcn_sched_barrier(0);
#pragma unroll
    for (int j = 0; j < NT; ++j) {
      if (j > 0) mmj(1, j);
      __builtin_amdgcn_sched_barrier(0);
      rdw(j, b ^ 1, 0);
      if (j < 4) rdx(0, a0, j);
      __builtin_amdgcn_sched_barrier(0);
    }
    xcur = xnext;
  };
#pragma unroll 1
  for (int t = 0; t < 10; t += 2) {
    half(0);
    half(1);
  }
  STAMP_AT(1, 3);
  lane = lane_id();

  if (ROLE == 0) {
    const unsigned so = AF_OFF + ((rt * 4 + tg) * NS2 + ct * 3) * 4096;
    u32x2 mid[4];
#pragma unroll
    for (int i = 0; i < 4; ++i) {
      unsigned hp[3][2];
#pragma unroll
      for (int u = 0; u < 3; ++u) {
        float hv[4];
#pragma unroll
        for (int r = 0; r < 4; ++r) {
          float gv = acc[i][2 * u][r], uv = acc[i][2 * u + 1][r];
          const float c1 = -2.f * 0.7978845608f * 1.4426950409f, c2 = c1 * 0.044715f;
          float e = __builtin_amdgcn_exp2f(gv * (c1 + c2 * gv * gv));
          hv[r] = gv * uv * __builtin_amdgcn_rcpf(1.f + e);
        }
        hp[u][0] = pack2(hv[0], hv[1]);
        hp[u][1] = pack2(hv[2], hv[3]);
      }
      const u32x4 full = ch ? u32x4{hp[1][0], hp[1][1], hp[2][0], hp[2][1]} : u32x4{hp[0][0], hp[0][1], hp[1][0], hp[1][1]};
      mid[i] = ch ? u32x2{hp[0][0], hp[0][1]} : u32x2{hp[2][0], hp[2][1]};
      st16(rws, lane * 16 + i * 1024, so + ch * 8192, full);
      if (i & 1) {
        auto s0 = __builtin_amdgcn_permlane32_swap(mid[i - 1][0], mid[i][0], false, false);
        auto s1 = __builtin_amdgcn_permlane32_swap(mid[i - 1][1], mid[i][1], false, false);
        st16(rws, (lane & 31) * 16 + (lane >> 5) * 1024 + (i - 1) * 1024, so + 4096 + ch * 512, u32x4{s0[0], s1[0], s0[1], s1[1]});
      }
    }
    if (arrive_last(smem + CNT, 8) && lane < 32) post(ws, bid2(rt * 4 + (lane & 3), lane >> 2), SL_H + ct, tag);
  } else if (ROLE == 1) {
#pragma unroll
    for (int ip = 0; ip < 2; ++ip) {
      const unsigned so = PQ_OFF + (((rt * 4 + tg) * 2 + ip) * 160 + ct * 40 + ch * 20) * 256;
#pragma unroll
      for (int j = 0; j < 5; ++j) {
        const f32x4 a = acc[2 * ip][j], b = acc[2 * ip + 1][j];
        st16(rws, lane * 16 + j * 1024, so,
             u32x4{pack2(phi_f(a[0]), phi_f(a[1])), pack2(phi_f(a[2]), phi_f(a[3])), pack2(phi_f(b[0]), phi_f(b[1])), pack2(phi_f(b[2]), phi_f(b[3]))});
      }
    }
    if (arrive_last(smem + CNT, 8) && lane < 8) post(ws, bid2(rt * 4 + (lane & 3), ct * 2 + (lane >> 2)), SL_Q, tag);
  } else {
    bf16x8 op[2][5];
#pragma unroll
    for (int hh = 0; hh < 2; ++hh)
#pragma unroll
      for (int j = 0; j < 5; ++j) {
        f32x4 a = acc[2 * hh][j], b = acc[2 * hh + 1][j];
        const bool isk = j < 2 || (j == 4 && (lane & 15) < 8);
        float v[8];
#pragma unroll
        for (int r = 0; r < 4; ++r) { v[r] = a[r]; v[4 + r] = b[r]; }
        if (j < 2 || j == 4) {
#pragma unroll
          for (int r = 0; r < 8; ++r) { float ph = phi_f(v[r]); v[r] = isk ? ph : v[r]; }
        }
        u32x4 pk = {pack2(v[0], v[1]), pack2(v[2], v[3]), pack2(v[4], v[5]), pack2(v[6], v[7])};
        op[hh][j] = __builtin_bit_cast(bf16x8, pk);
      }
    const u32x4 onesu = {0x3f803f80u, 0x3f803f80u, 0x3f803f80u, 0x3f803f80u};
    const bf16x8 ones = __builtin_bit_cast(bf16x8, onesu);
    f32x4 z[10];
#pragma unroll
    for (int n = 0; n < 10; ++n) z[n] = f32x4{0.f, 0.f, 0.f, 0.f};
#pragma unroll
    for (int hh = 0; hh < 2; ++hh) {
      z[0] = mfma16(op[hh][0], op[hh][2], z[0]);
      z[1] = mfma16(op[hh][1], op[hh][3], z[1]);
      z[2] = mfma16(op[hh][0], op[hh][4], z[2]);
      z[3] = mfma16(op[hh][1], op[hh][4], z[3]);
      z[4] = mfma16(op[hh][4], op[hh][2], z[4]);
      z[5] = mfma16(op[hh][4], op[hh][3], z[5]);
      z[6] = mfma16(op[hh][4], op[hh][4], z[6]);
      z[7] = mfma16(op[hh][0], ones, z[7]);
      z[8] = mfma16(op[hh][1], ones, z[8]);
      z[9] = mfma16(op[hh][4], ones, z[9]);
    }
    f32x4* red = (f32x4*)smem;
#pragma unroll
    for (int n = 0; n < 10; ++n) red[(wid * 10 + n) * 64 + lane] = z[n];
    asm volatile("s_waitcnt lgkmcnt(0)" ::: "memory");
    bar();
    const int tid = wid * 64 + lane;
    if (tid < 420) {
      const int hh = tid / 105, rem = tid % 105, e = rem / 5, dq = rem % 5, wh = hh & 1;
      int n, ln;
      if (dq < 4) { n = e < 16 ? wh : e < 20 ? 2 + wh : 7 + wh; ln = dq * 16; }
      else { n = e < 16 ? 4 + wh : e < 20 ? 6 : 9; ln = wh * 16; }
      ln += e < 16 ? e : e < 20 ? 8 + 4 * wh + (e - 16) : 0;
      const f32x4* src = red + ((hh >> 1) * 40 + n) * 64 + ln;
      const f32x4 s = (src[0] + src[640]) + (src[1280] + src[1920]);
      st16(rws, tid * 16, KVP_OFF + (rt * 32 + ct * 4) * 1680, __builtin_bit_cast(u32x4, s));
    }
    asm volatile("s_waitcnt lgkmcnt(0)" ::: "memory");
    bar();
    if (arrive_last(smem + CNT, 8) && lane < 32) post(ws, bid2(lane, ct), SL_KV + rt, tag);
  }
}

DEVI int w2_row(int s, int g, int half) {
  if (s < 54) return s % 3 == 1 ? s * 32 + 16 * (g >> 1) + 4 * (g & 1) + 8 * half : s * 32 + 4 * g + 16 * half;
  const int chn = (s - 54) * 4 + g, c = chn / 10, i = chn % 10;
  return F_ + 80 * c + (i < 8 ? 20 * (i >> 1) + 4 * (i & 1) + 8 * half : 40 * (i - 8) + 16 + 20 * half);
}
DEVI void relayout(char* smem, rsrc_t rws, const char* __restrict__ wd, const char* __restrict__ wo, char* ws, int id, int wid, int lane, u64 tag) {
  const int g = lane >> 4, q = (lane & 15) >> 2, p = lane & 3;
#pragma unroll 1
  for (int s0 = id; s0 < NS2; s0 += 48) {
#pragma unroll
    for (int u = 0; u < 3; ++u) {
      const int s = s0 + u * 16;
      if (s < NS2) {
        const rsrc_t rs = s < 54 ? mk_rsrc(wd, F_ * 1280) : mk_rsrc(wo - F_ * 1280, (F_ + D_) * 1280);
        const int row0 = w2_row(s, wid >> 1, wid & 1);
#pragma unroll
        for (int i = 0; i < 5; ++i) dma16<0>(rs, smem + u * 40960 + (wid * 5 + i) * 1024, i * 1024 + lane * 16, row0 * 1280);
      }
    }
    drain();
    bar();
#pragma unroll
    for (int u = 0; u < 3; ++u) {
      const int s = s0 + u * 16;
      if (s < NS2) {
#pragma unroll
        for (int j = 0; j < 5; ++j) {
          const char* pp = smem + u * 40960 + (g * 8 + q) * 1280 + (wid * 5 + j) * 32 + p * 8;
          const bf16x8 v = cat8(tr_read(pp), tr_read(pp + 4 * 1280));
          st16(rws, lane * 16 + j * 1024, WF_OFF + (wid * NS2 + s) * 5120, __builtin_bit_cast(u32x4, v));
        }
      }
    }
    drain();
    bar();
  }
  if (wid < 4) post(ws, wid * 64 + lane, SL_RL + id, tag);
}

DEVI void o_compute(char* scr, rsrc_t rws, int r, int c, int lw, int lane) {
  const int fr = lane & 15, g = lane >> 4, head = c * 4 + lw;
  u32x4 pa[8], pb[8], ql[2], qh[2];
#pragma unroll
  for (int p = 0; p < 8; ++p) {
    pa[p] = ld16(rws, lane * 16, KVP_OFF + (p * 32 + head) * 1680);
    pb[p] = ld16(rws, 1024 + (lane < 41 ? lane : 40) * 16, KVP_OFF + (p * 32 + head) * 1680);
  }
#pragma unroll
  for (int ip = 0; ip < 2; ++ip) {
    ql[ip] = ld16(rws, lane * 16, PQ_OFF + ((r * 2 + ip) * 160 + head * 5) * 256);
    qh[ip] = ld16(rws, 1024 + fr * 16, PQ_OFF + ((r * 2 + ip) * 160 + head * 5) * 256);
  }
  f32x4 sa = __builtin_bit_cast(f32x4, pa[0]), sb = __builtin_bit_cast(f32x4, pb[0]);
#pragma unroll
  for (int p = 1; p < 8; ++p) { sa += __builtin_bit_cast(f32x4, pa[p]); sb += __builtin_bit_cast(f32x4, pb[p]); }
  *(u32x2*)(scr + lane * 8) = u32x2{pack2(sa[0], sa[1]), pack2(sa[2], sa[3])};
  *(u32x2*)(scr + 512 + lane * 8) = u32x2{pack2(sb[0], sb[1]), pack2(sb[2], sb[3])};
  auto frag = [&](int e) {
    const u32x2 lo = *(const u32x2*)(scr + e * 40 + g * 8);
    u32x2 hi = *(const u32x2*)(scr + e * 40 + 32);
    if (g != 0) hi = u32x2{0u, 0u};
    return __builtin_bit_cast(bf16x8, u32x4{lo[0], lo[1], hi[0], hi[1]});
  };
  const bf16x8 A0 = frag(fr), A1 = frag(16 + (fr & 3)), A2 = frag(20);
  u32x2 main_[4], last[4];
#pragma unroll
  for (int i = 0; i < 4; ++i) {
    const u32x4 l = ql[i >> 1], h = qh[i >> 1];
    const unsigned h0 = g == 0 ? h[(i & 1) * 2] : 0u, h1 = g == 0 ? h[(i & 1) * 2 + 1] : 0u;
    const bf16x8 B = __builtin_bit_cast(bf16x8, u32x4{l[(i & 1) * 2], l[(i & 1) * 2 + 1], h0, h1});
    const f32x4 zz = {0, 0, 0, 0};
    const f32x4 n0 = mfma16(A0, B, zz), n1 = mfma16(A1, B, zz), dn = mfma16(A2, B, zz);
    const float inv = 1.f / (dn[0] + 1e-6f);
    main_[i] = u32x2{pack2(n0[0] * inv, n0[1] * inv), pack2(n0[2] * inv, n0[3] * inv)};
    last[i] = u32x2{pack2(n1[0] * inv, n1[1] * inv), pack2(n1[2] * inv, n1[3] * inv)};
  }
  const unsigned so = AF_OFF + (r * NS2 + 54) * 4096;
  {
    const int chn = c * 10 + lw * 2 + (g & 1);
    const unsigned vo = (chn >> 2) * 4096 + (chn & 3) * 256 + fr * 16 + (g >> 1) * 1024;
#pragma unroll
    for (int ip = 0; ip < 2; ++ip) {
      auto s0 = __builtin_amdgcn_permlane32_swap(main_[2 * ip][0], main_[2 * ip + 1][0], false, false);
      auto s1 = __builtin_amdgcn_permlane32_swap(main_[2 * ip][1], main_[2 * ip + 1][1], false, false);
      st16(rws, vo + ip * 2048, so, u32x4{s0[0], s1[0], s0[1], s1[1]});
    }
  }
  {
    const int chn = c * 10 + 8 + (lw >> 1);
    const u32x2 v = g == 0 ? last[0] : g == 1 ? last[1] : g == 2 ? last[2] : last[3];
    st8(rws, (chn >> 2) * 4096 + (chn & 3) * 256 + fr * 16 + g * 1024 + (lw & 1) * 8, so, v);
  }
}

constexpr int RD = 12;
__global__ __launch_bounds__(768, 3) void fused(const char* __restrict__ x, const char* __restrict__ wqkv, const char* __restrict__ wo,
                                                const char* __restrict__ wg, const char* __restrict__ wu, const char* __restrict__ wd,
                                                char* __restrict__ out, char* ws) {
  extern __shared__ __attribute__((aligned(16))) char smem[];
  STAMP_AT(1, 0);
  STAMP_AT(0, 0);
  int lane = threadIdx.x & 63;
  const int wid = __builtin_amdgcn_readfirstlane(threadIdx.x >> 6);
  if (threadIdx.x == 0) *(u32x2*)(smem + CNT) = u32x2{0u, 0u};
  const int bid = blockIdx.x, xcd = bid & 7, idx = bid >> 3;
  const int rt = (xcd & 1) * 4 + (idx & 3), cs = idx >> 2;
  const int ct = (((cs >> 1) * 4 + (xcd >> 1)) << 1) + (cs & 1);
  const int r2 = xcd * 4 + (idx & 3), c2 = idx >> 2;
  const rsrc_t rws = mk_rsrc(ws, WS_END);
  const char* box = ws + BOX_OFF + bid * 1024;

  if (wid < 8) {
    const int row = bid * 8 + wid;
    const u64 gen = __hip_atomic_load((const GAS u64*)(ws + GEN_OFF), RLX_AGENT);
    const u32x4 v0 = *(const u32x4*)(x + row * 1280 + lane * 16);
    u32x4 v1 = *(const u32x4*)(x + row * 1280 + 1024 + (lane & 15) * 16);
    __builtin_amdgcn_sched_barrier(0);
    bar();
    if (lane >= 16) v1 = u32x4{0u, 0u, 0u, 0u};
    float s = 0.f;
#pragma unroll
    for (int i = 0; i < 4; ++i) s += (bf_lo(v0[i]) + bf_hi(v0[i])) + (bf_lo(v1[i]) + bf_hi(v1[i]));
#pragma unroll
    for (int o = 32; o; o >>= 1) s += __shfl_xor(s, o);
    const float m = s * (1.f / 640.f);
    float q = 0.f, q1 = 0.f;
#pragma unroll
    for (int i = 0; i < 4; ++i) {
      float a = bf_lo(v0[i]) - m, b = bf_hi(v0[i]) - m, c = bf_lo(v1[i]) - m, d = bf_hi(v1[i]) - m;
      q += a * a + b * b;
      q1 += c * c + d * d;
    }
    if (lane < 16) q += q1;
#pragma unroll
    for (int o = 32; o; o >>= 1) q += __shfl_xor(q, o);
    const float rs = rsqrtf(q * (1.f / 640.f) + 1e-5f);
    u32x4 o0, o1;
#pragma unroll
    for (int i = 0; i < 4; ++i) {
      o0[i] = pack2((bf_lo(v0[i]) - m) * rs, (bf_hi(v0[i]) - m) * rs);
      o1[i] = pack2((bf_lo(v1[i]) - m) * rs, (bf_hi(v1[i]) - m) * rs);
    }
    const int rr = row & 255;
    const unsigned vo = (lane >> 3) * 32768 + (((lane & 7) ^ ((rr >> 1) & 7)) << 4), so = XN_OFF + (row >> 8) * 327680 + rr * 128;
    st16(rws, vo, so, o0);
    if (lane < 16) st16(rws, vo + 8 * 32768, so, o1);
    const u64 tag = uni64(gen) + 1;
    if (threadIdx.x == 0) *(u64*)(smem + CNT + 8) = tag;
    asm volatile("s_waitcnt vmcnt(0) lgkmcnt(0)" ::: "memory");
    bar();
    if (wid == 0) {
      if (lane < 30) post(ws, bid1(bid >> 5, lane), SL_XN + (bid & 31), tag);
      if (lane == 32) __hip_atomic_store((GAS u64*)(ws + ALL_OFF + bid * 8), tag, RLX_AGENT);
    }
    STAMP_AT(1, 1);
    if (ct < 18) g1_compute<0>(smem, rws, ws, rt, ct, wid, lane, tag);
    else if (ct < 22) g1_compute<1>(smem, rws, ws, rt, ct - 18, wid, lane, tag);
    else if (ct < 30) g1_compute<2>(smem, rws, ws, rt, ct - 22, wid, lane, tag);
    else relayout(smem, rws, wd, wo, ws, (ct - 30) * 8 + rt, wid, lane, tag);
    STAMP_AT(1, 4);

    lane = lane_id();
    const int kq = wid & 3, th = wid >> 2;
    f32x4 acc[2][5];
#pragma unroll
    for (int i = 0; i < 2; ++i)
#pragma unroll
      for (int j = 0; j < 5; ++j) acc[i][j] = f32x4{0.f, 0.f, 0.f, 0.f};
    u32x4 af[RD][2];
    bf16x8 wf[5];
    const unsigned aso = AF_OFF + (r2 * NS2 + kq) * 4096 + th * 2048;
    auto lda = [&](int gI) {
      const unsigned so = aso + ((gI == 18 && kq >= 2) ? 17 : gI) * 16384;
      af[gI % RD][0] = ld16(rws, lane * 16, so);
      af[gI % RD][1] = ld16(rws, lane * 16 + 1024, so);
    };
    const char* wl = smem + kq * 5120 + lane * 16;
    auto rdw = [&](int gI, int j) { wf[j] = *(const bf16x8*)(wl + (gI % 7) * 20480 + j * 1024); };
    bar();
    STAMP_AT(1, 5);
#pragma unroll
    for (int gI = 0; gI < RD; ++gI) lda(gI);
#pragma unroll
    for (int j = 0; j < 5; ++j) rdw(0, j);
    __builtin_amdgcn_sched_barrier(0);
#pragma unroll
    for (int gI = 0; gI < 19; ++gI) {
      if (gI == 18 && kq >= 2) break;
#pragma unroll
      for (int j = 0; j < 5; ++j) {
        acc[0][j] = mfma16(wf[j], __builtin_bit_cast(bf16x8, af[gI % RD][0]), acc[0][j]);
        acc[1][j] = mfma16(wf[j], __builtin_bit_cast(bf16x8, af[gI % RD][1]), acc[1][j]);
        __builtin_amdgcn_sched_barrier(0);
        if (j == 0 && (gI & 1) && gI < 16) {
          asm volatile("s_waitcnt lgkmcnt(0)" ::: "memory");
          bar();
        }
        if (gI + 1 < 19) rdw(gI + 1, j);
        __builtin_amdgcn_sched_barrier(0);
      }
      if (gI + RD < 19) lda(gI + RD);
      __builtin_amdgcn_sched_barrier(0);
    }
    asm volatile("s_waitcnt lgkmcnt(0)" ::: "memory");
    bar();
    {
      lane = lane_id();
      char* red = smem + kq * 21504 + (th * 32 + (lane & 15)) * 336 + (lane >> 4) * 16;
#pragma unroll
      for (int i = 0; i < 2; ++i)
#pragma unroll
        for (int j = 0; j < 5; ++j) *(f32x4*)(red + i * 16 * 336 + j * 64) = acc[i][j];
    }
  } else {
    const int lw = wid - 8;
    if (ct < 18) g1_loader<0>(smem, rws, wg, wu, ws, rt, ct, lw, lane);
    else if (ct < 22) g1_loader<1>(smem, rws, wqkv, wqkv, ws, rt, ct - 18, lw, lane);
    else if (ct < 30) g1_loader<2>(smem, rws, wqkv, wqkv, ws, rt, ct - 22, lw, lane);
    else { bar(); bar(); }
    const u64 tag = uni64(*(const u64*)(smem + CNT + 8));
    auto ldw = [&](int gI) {
      const int s = (gI == 18 && lw >= 2) ? 72 : 4 * gI + lw;
#pragma unroll
      for (int j = 0; j < 5; ++j) dma16<16>(rws, smem + ((gI % 7) * 4 + lw) * 5120 + j * 1024, lane * 16, WF_OFF + (c2 * NS2 + s) * 5120 + j * 1024);
    };
    if (ct < 30) {
      await(box + SL_RL * 8, 16, tag, lane);
      drain();
      bar();
      if (ct < 22) { ldw(2); ldw(3); ldw(4); ldw(5); }
      bar();
      if (ct < 22) { ldw(0); ldw(1); ldw(6); }
      else { bar(); bar(); }
      STAMP_AT(0, 3);
    } else {
      if (bid == 255 && lw == 0) {
#pragma unroll 1
        for (int k = 0; k < 4; ++k) await(ws + ALL_OFF + k * 512, 64, tag, lane);
        if (lane == 0) __hip_atomic_store((GAS u64*)(ws + GEN_OFF), tag, RLX_AGENT);
      }
      const int id = (ct - 30) * 8 + rt;
      for (int s0 = id; s0 < NS2; s0 += 48) { bar(); bar(); }
      await(box + SL_RL * 8, 16, tag, lane);
    }
    if (ct >= 22) {
#pragma unroll
      for (int gI = 0; gI < 7; ++gI) ldw(gI);
    }
    await(box + SL_Q * 8, 9, tag, lane);
    STAMP_AT(0, 4);
    o_compute(smem + OSCR + lw * 1792, rws, r2, c2, lw, lane);
    if (arrive_last(smem + CNT + 4, 4) && lane < 8) post(ws, bid2(r2, lane), SL_O + c2, tag);
    STAMP_AT(0, 5);
    if (lw == 0) await(box + SL_H * 8, 26, tag, lane);
    drain();
    STAMP_AT(0, 6);
    bar();
#pragma unroll
    for (int k = 0; k < 8; ++k) {
      if (k >= 2) { if (k < 7) asm volatile("s_waitcnt vmcnt(10)" ::: "memory"); else drain(); }
      bar();
      if (k < 6) { ldw(2 * k + 7); ldw(2 * k + 8); }
    }
    bar();
  }
  const int tid = wid * 64 + lane_id();
  const int tok = tid / 10, m8 = tid % 10;
  const unsigned oo = (r2 * 64 + tok) * 1280 + c2 * 160 + m8 * 16;
  u32x4 xr = {0u, 0u, 0u, 0u};
  if (tid < 640) xr = *(const u32x4*)(x + oo);
  asm volatile("s_waitcnt lgkmcnt(0)" ::: "memory");
  bar();
  if (tid < 640) {
    const char* rp = smem + tok * 336 + m8 * 32;
    f32x4 lo = *(const f32x4*)rp, hi = *(const f32x4*)(rp + 16);
#pragma unroll
    for (int k = 1; k < 4; ++k) { lo += *(const f32x4*)(rp + k * 21504); hi += *(const f32x4*)(rp + k * 21504 + 16); }
    *(u32x4*)(out + oo) = u32x4{pack2(lo[0] + bf_lo(xr[0]), lo[1] + bf_hi(xr[0])), pack2(lo[2] + bf_lo(xr[1]), lo[3] + bf_hi(xr[1])),
                                pack2(hi[0] + bf_lo(xr[2]), hi[1] + bf_hi(xr[2])), pack2(hi[2] + bf_lo(xr[3]), hi[3] + bf_hi(xr[3]))};
  }
  STAMP_AT(1, 6);
  STAMP_AT(0, 7);
}

extern "C" void kernel_launch(void* const* d_in, const int* in_sizes, int n_in, void* d_out, int out_size, void* d_ws,
                              size_t ws_size, hipStream_t stream) {
  (void)hipFuncSetAttribute((const void*)fused, hipFuncAttributeMaxDynamicSharedMemorySize, LDS_BYTES);
  fused<<<256, 768, LDS_BYTES, stream>>>((const char*)d_in[0], (const char*)d_in[1], (const char*)d_in[2], (const char*)d_in[4],
                                         (const char*)d_in[5], (const char*)d_in[6], (char*)d_out, (char*)d_ws);
}
```

```cpp
#define PROBE 0
#include <hip/hip_runtime.h>
#include <hip/hip_bf16.h>
#include <stdint.h>

typedef __attribute__((ext_vector_type(8))) __bf16 bf16x8;
typedef __attribute__((ext_vector_type(2))) __bf16 bf16x2;
typedef __attribute__((ext_vector_type(4))) short s16x4;
typedef __attribute__((ext_vector_type(4))) float f32x4;
typedef __attribute__((ext_vector_type(2))) float f32x2;
typedef __attribute__((ext_vector_type(4))) unsigned u32x4;
typedef __attribute__((ext_vector_type(2))) unsigned u32x2;
typedef unsigned long long u64;
#define LAS __attribute__((address_space(3)))
#define GAS __attribute__((address_space(1)))
#define DEVI __device__ __forceinline__
#define RLX_AGENT __ATOMIC_RELAXED, __HIP_MEMORY_SCOPE_AGENT
typedef __amdgpu_buffer_rsrc_t rsrc_t;

constexpr int S_ = 2048, D_ = 640, F_ = 1728;
constexpr int NS2 = 74;
constexpr unsigned GEN_OFF = 0;
constexpr unsigned ALL_OFF = 4096;
constexpr unsigned BOX_OFF = 8192;
constexpr unsigned XN_OFF = BOX_OFF + 256 * 2048;
constexpr unsigned AF_OFF = XN_OFF + S_ * D_ * 2;
constexpr unsigned WF_OFF = AF_OFF + 32 * NS2 * 4096;
constexpr unsigned KVP_OFF = WF_OFF + 8 * NS2 * 5120;
constexpr unsigned ZP_OFF = KVP_OFF + 64 * 4096;
constexpr unsigned ZF_OFF = ZP_OFF + 64 * 384;
constexpr unsigned WS_END = ZF_OFF + 64 * 384;
constexpr unsigned STAMP_OFF = (WS_END + 4095) & ~4095u;
constexpr int SL_XN = 0, SL_H = 32, SL_O = 50, SL_KV = 64, SL_RL = 128, SL_M = 144;

#define STAMP_AT(k, i) do {} while (0)
#define HSTAMP
#define PSTAMP(i) do {} while (0)

DEVI float bf_lo(unsigned v) { return __builtin_bit_cast(float, v << 16); }
DEVI float bf_hi(unsigned v) { return __builtin_bit_cast(float, v & 0xffff0000u); }
DEVI unsigned pack2(float a, float b) {
  f32x2 f = {a, b};
  return __builtin_bit_cast(unsigned, __builtin_convertvector(f, bf16x2));
}
DEVI rsrc_t mk_rsrc(const void* p, unsigned bytes) { return __builtin_amdgcn_make_buffer_rsrc((void*)p, 0, bytes, 0x00020000); }
DEVI f32x4 mfma16(bf16x8 a, bf16x8 b, f32x4 c) { return __builtin_amdgcn_mfma_f32_16x16x32_bf16(a, b, c, 0, 0, 0); }
DEVI s16x4 tr_read(const char* p) { return __builtin_amdgcn_ds_read_tr16_b64_v4i16((LAS s16x4*)p); }
DEVI bf16x8 cat8(s16x4 a, s16x4 b) {
  typedef __attribute__((ext_vector_type(8))) short s16x8;
  s16x8 r = __builtin_shufflevector(a, b, 0, 1, 2, 3, 4, 5, 6, 7);
  return __builtin_bit_cast(bf16x8, r);
}
DEVI float exp2_le1(float t) { return __builtin_amdgcn_fmed3f(__builtin_amdgcn_exp2f(t), 0.f, 1.f); }
DEVI f32x2 phi2(f32x2 t) {
  const f32x2 s = t * f32x2{1.4426950409f, 1.4426950409f};
  const f32x2 u = {__builtin_elementwise_maximum(t[0], 1e-6f), __builtin_elementwise_maximum(t[1], 1e-6f)};
  return u + f32x2{exp2_le1(s[0]), exp2_le1(s[1])};
}
DEVI unsigned sel(unsigned m, unsigned a, unsigned b) { return (m & a) | (~m & b); }
DEVI f32x4 sel4(unsigned m, f32x4 a, f32x4 b) {
  const u32x4 x = __builtin_bit_cast(u32x4, a), y = __builtin_bit_cast(u32x4, b);
  return __builtin_bit_cast(f32x4, u32x4{sel(m, x[0], y[0]), sel(m, x[1], y[1]), sel(m, x[2], y[2]), sel(m, x[3], y[3])});
}
DEVI unsigned bitmask(int lane, int bit) { unsigned m = (unsigned)(((int)((unsigned)lane << (31 - bit))) >> 31); asm volatile("" : "+v"(m)); return m; }
DEVI f32x4 phi4(f32x4 t) {
  const f32x2 a = phi2(f32x2{t[0], t[1]}), b = phi2(f32x2{t[2], t[3]});
  return f32x4{a[0], a[1], b[0], b[1]};
}
DEVI u32x2 pack4(f32x4 v) { return u32x2{pack2(v[0], v[1]), pack2(v[2], v[3])}; }
template <int AUX> DEVI void dma16(rsrc_t rs, char* lds, unsigned voff, unsigned soff) {
  __builtin_amdgcn_raw_ptr_buffer_load_lds(rs, (LAS void*)lds, 16, voff, soff, 0, AUX);
}
DEVI void st16(rsrc_t rs, unsigned voff, unsigned soff, u32x4 v) { __builtin_amdgcn_raw_buffer_store_b128(v, rs, voff + soff, 0, 16); }
DEVI void st8(rsrc_t rs, unsigned voff, unsigned soff, u32x2 v) { __builtin_amdgcn_raw_buffer_store_b64(v, rs, voff + soff, 0, 16); }
DEVI u32x4 ld16(rsrc_t rs, unsigned voff, unsigned soff) { return __builtin_amdgcn_raw_buffer_load_b128(rs, voff, soff, 16); }
DEVI void drain() { asm volatile("s_waitcnt vmcnt(0)" ::: "memory"); }
DEVI void bar() { __builtin_amdgcn_sched_barrier(0); asm volatile("s_barrier" ::: "memory"); __builtin_amdgcn_sched_barrier(0); }
DEVI void post(char* ws, int consumer, int slot, u64 tag) {
  __hip_atomic_store((GAS u64*)(ws + BOX_OFF + consumer * 2048 + slot * 8), tag, RLX_AGENT);
}
#define PSL 4
template <int SL = 1> DEVI void await(const char* p, int n, u64 tag, int lane) {
  const GAS u64* q = (const GAS u64*)(p) + (lane < n ? lane : 0);
#pragma unroll 1
  for (int spin = 0; spin < (1 << 18); ++spin) {
    u64 v = __hip_atomic_load(q, RLX_AGENT);
    if (__all(v == tag)) break;
    __builtin_amdgcn_s_sleep(SL);
  }
  __builtin_amdgcn_fence(__ATOMIC_ACQUIRE, "wavefront");
}
template <int CTRL> DEVI float dpp_add(float v) {
  return v + __builtin_bit_cast(float, __builtin_amdgcn_update_dpp(0, __builtin_bit_cast(int, v), CTRL, 0xf, 0xf, true));
}
DEVI float rdl(float v, int l) { return __builtin_bit_cast(float, __builtin_amdgcn_readlane(__builtin_bit_cast(int, v), l)); }
DEVI float wave_sum(float v) {
  v = dpp_add<0xB1>(v);
  v = dpp_add<0x4E>(v);
  v = dpp_add<0x141>(v);
  v = dpp_add<0x140>(v);
  return (rdl(v, 0) + rdl(v, 16)) + (rdl(v, 32) + rdl(v, 48));
}
DEVI int lane_id() { int l; asm volatile("v_mbcnt_lo_u32_b32 %0, -1, 0\n\tv_mbcnt_hi_u32_b32 %0, -1, %0" : "=v"(l)); return l; }
template <int KEEP = 0> DEVI bool arrive_last(char* cnt, unsigned n) {
  asm volatile("s_waitcnt vmcnt(%0)" :: "n"(KEEP) : "memory");
  unsigned old = 0;
  if (lane_id() == 0) old = __hip_atomic_fetch_add((LAS unsigned*)cnt, 1u, __ATOMIC_RELAXED, __HIP_MEMORY_SCOPE_WORKGROUP);
  return __builtin_amdgcn_readfirstlane(old) == n - 1;
}
DEVI u64 uni64(u64 v) { return ((u64)(unsigned)__builtin_amdgcn_readfirstlane((unsigned)(v >> 32)) << 32) | (unsigned)__builtin_amdgcn_readfirstlane((unsigned)v); }
DEVI int bid1(int rt, int ct) {
  const int b = ct < 16 ? ct >> 2 : ct < 18 ? 2 * (ct - 16) + ((rt >> 1) & 1) : ct < 22 ? ct - 18 : (ct - 22) >> 1;
  const int idx = ct < 16 ? (ct & 3) * 4 + (rt & 3) : ct < 18 ? 28 + (rt & 1) : ct < 22 ? 16 + (rt & 3) : 20 + (ct & 1) * 4 + (rt & 3);
  return (idx << 3) | (b << 1) | (rt >> 2);
}
DEVI int bid_spare(int id) { return ((30 + (id & 1)) << 3) | (id >> 1); }
DEVI int bid2(int r, int c) { return (((r & 7) * 4 + (c & 3)) << 3) | ((r >> 3) << 1) | (c >> 2); }

#define KVREP 32
constexpr int XB = 32768, WB = 24576;
constexpr int WBASE = 3 * XB, MISC = 3 * XB + 2 * WB;
constexpr int ZBUF = MISC + 256;
constexpr int WOB = ZBUF + 2560;
constexpr int KVI = 143360;
constexpr int CNT = WOB + 13312;
constexpr int C_EPI = CNT, C_OQ = CNT + 4, C_TAG = CNT + 8, C_DONE1 = CNT + 16, C_KVB = CNT + 20, C_KVD = CNT + 24, C_H = CNT + 28,
              C_FULL = CNT + 32, C_FREE = CNT + 48, C_QK = CNT + 80, C_MB = CNT + 84, C_M = CNT + 88, C_WO = CNT + 92, C_ZK = CNT + 96;
constexpr int LDS_BYTES = CNT + 112;
static_assert(LDS_BYTES <= 163840, "LDS");
DEVI unsigned lds_ld(const char* p) { return __hip_atomic_load((const LAS unsigned*)p, __ATOMIC_RELAXED, __HIP_MEMORY_SCOPE_WORKGROUP); }
DEVI void lds_st(char* p, unsigned v) { asm volatile("ds_write_b32 %0, %1" :: "v"((unsigned)(size_t)(LAS char*)p), "v"(v) : "memory"); }
DEVI unsigned lds_ld_now(const char* p) {
  unsigned v;
  asm volatile("ds_read_b32 %0, %1\n\ts_waitcnt lgkmcnt(0)" : "=v"(v) : "v"((unsigned)(size_t)(const LAS char*)p) : "memory");
  return v;
}
DEVI void lds_inc(char* p) {
  asm volatile("" ::: "memory");
  if (lane_id() == 0) __hip_atomic_fetch_add((LAS unsigned*)p, 1u, __ATOMIC_RELAXED, __HIP_MEMORY_SCOPE_WORKGROUP);
  asm volatile("" ::: "memory");
}
DEVI void lds_wait(const char* p, unsigned n) {
#pragma unroll 1
  while ((unsigned)__builtin_amdgcn_readfirstlane(lds_ld_now(p)) < n) __builtin_amdgcn_s_sleep(1);
  asm volatile("" ::: "memory");
}
DEVI u32x4 pair_mid(u32x2 a, u32x2 b) {
  auto s0 = __builtin_amdgcn_permlane32_swap(a[0], b[0], false, false);
  auto s1 = __builtin_amdgcn_permlane32_swap(a[1], b[1], false, false);
  return u32x4{s0[0], s1[0], s0[1], s1[1]};
}

template <class T>
DEVI void g1_loader(char* smem, rsrc_t rws, const char* __restrict__ wsrc, char* ws, int rt, int ct, int lw, int lane, T touch) {
  const bool gu = ct < 18;
  const int wid = lw + 8; (void)wid;
  const unsigned wstr = gu ? F_ * 2 : 1920 * 2;
  const rsrc_t rw = mk_rsrc(wsrc, D_ * wstr);
  unsigned wo_[6];
#pragma unroll
  for (int i = 0; i < 6; ++i) {
    int r, colb;
    if (gu) {
      const int ci = ((lw & 1) * 6 + i) * 64 + lane;
      r = ci / 12;
      colb = ct * 192 + ((ci % 12) ^ (((r >> 3) & 1) << 1)) * 16;
    } else {
      const int ci = (lw * 5 + i) * 64 + lane;
      r = ci / 20;
      const int lc = (ci % 20) ^ (((r >> 3) & 1) << 1);
      colb = ct < 22 ? (ct - 18) * 320 + lc * 16 : (lc < 10 ? 1280 : 2560 - 160) + (ct - 22) * 160 + lc * 16;
    }
    wo_[i] = r * wstr + colb;
  }
  char* wdst = smem + WBASE + lw * (gu ? 6144 : 5120);
  auto stage_x = [&](int t, unsigned xb) {
#pragma unroll
    for (int n = 0; n < 8; ++n) dma16<16>(rws, smem + xb + (lw * 8 + n) * 1024, lane * 16, XN_OFF + (rt * 10 + t) * 32768 + (lw * 8 + n) * 1024);
  };
  auto stage_w = [&](int t, int b) {
#pragma unroll
    for (int n = 0; n < 6; ++n)
      if (n < 5 || gu) dma16<0>(rw, wdst + b * WB + n * 1024, wo_[n], t * 64 * wstr);
  };
  bar();
  __builtin_amdgcn_s_sleep(1);
  stage_w(0, 0);
  stage_w(1, 1);
  STAMP_AT(0, 1);
  const u64 tag = uni64(*(const u64*)(smem + C_TAG));
  await(ws + BOX_OFF + blockIdx.x * 2048 + SL_XN * 8, 32, tag, lane);
  STAMP_AT(0, 2);
  stage_x(0, 0); stage_x(1, XB);
  asm volatile("s_waitcnt vmcnt(8)" ::: "memory");
  bar();
  touch(4 + lw);
  stage_x(2, 2 * XB);
  unsigned xb = 0;
#pragma unroll 1
  for (int t = 0; t < 8; ++t) {
    if (t == 0 || (t == 3 && lw < 2)) asm volatile("s_waitcnt vmcnt(9)" ::: "memory");
    else asm volatile("s_waitcnt vmcnt(8)" ::: "memory");
    bar();
    stage_w(t + 2, t & 1);
    if (t + 3 < 10) stage_x(t + 3, xb);
    if (t == 2 && lw < 2) touch(8 + lw);
    xb = xb == 2 * XB ? 0 : xb + XB;
  }
}

template <int ROLE>
DEVI void g1_compute(char* smem, rsrc_t rws, char* ws, int rt, int ct, int wid, int lane, u64 tag) {
  constexpr int NT = ROLE == 0 ? 6 : 5;
  constexpr int ROWB = ROLE == 0 ? 192 : 320;
  const int tg = wid & 3, ch = wid >> 2;
  const int fr = lane & 15, g = lane >> 4;
  unsigned xa[2];
#pragma unroll
  for (int kk = 0; kk < 2; ++kk) xa[kk] = (tg * 64 + fr) * 128 + (((kk * 4 + g) ^ (fr >> 1)) << 4);
  unsigned wa[NT];
  {
    const int q = (lane & 15) >> 2, p = lane & 3;
#pragma unroll
    for (int j = 0; j < NT; ++j) {
      int blk = 0, c;
      if (ROLE == 0) { blk = (j & 1) * 12288; c = (j < 4 ? ch * 4 + (j >> 1) : 2 + ch) * 32 + p * 8; }
      else if (ROLE == 1) c = ch * 160 + p * 40 + j * 8;
      else {
        if (j < 4) c = (j >> 1) * 160 + (ch * 2 + (j & 1)) * 40 + p * 8;
        else c = (p >> 1) * 160 + (ch * 2 + (p & 1)) * 40 + 32;
      }
      c ^= (g & 1) << 5;
      wa[j] = WBASE + blk + (g * 8 + q) * ROWB + c;
    }
  }
  f32x4 acc[4][NT];
#pragma unroll
  for (int i = 0; i < 4; ++i)
#pragma unroll
    for (int j = 0; j < NT; ++j) {
      const float a0 = (ROLE == 1 || (ROLE == 2 && j < 2)) ? 1e-6f : (ROLE == 2 && j == 4 && fr < 8) ? 1e-6f : 0.f;
      acc[i][j] = f32x4{a0, a0, a0, a0};
    }
  bf16x8 xf[2][4], wf[NT];
  auto rdw = [&](int j, int b, int kk) {
    const char* p = smem + b * WB + wa[j] + kk * 32 * ROWB;
    wf[j] = cat8(tr_read(p), tr_read(p + 4 * ROWB));
  };
  auto rdx = [&](int set, unsigned xaddr, int i) { xf[set][i] = *(const bf16x8*)(smem + xaddr + i * 2048); };
  auto mmj = [&](int set, int j) {
#pragma unroll
    for (int i = 0; i < 4; ++i)
      acc[i][j] = ROLE == 2 ? mfma16(xf[set][i], wf[j], acc[i][j]) : mfma16(wf[j], xf[set][i], acc[i][j]);
  };
  bar();
  STAMP_AT(1, 2);
#pragma unroll
  for (int i = 0; i < 4; ++i) rdx(0, xa[0], i);
#pragma unroll
  for (int j = 0; j < NT; ++j) rdw(j, 0, 0);
  __builtin_amdgcn_sched_barrier(0);
  unsigned xcur = 0;
  auto half = [&](int b, int skip) {
    const unsigned xnext = xcur == 2 * XB ? 0 : xcur + XB;
    const unsigned a1 = xa[1] + xcur, a0 = xa[0] + xnext;
#pragma unroll
    for (int j = 0; j < NT; ++j) {
      mmj(0, j);
      __builtin_amdgcn_sched_barrier(0);
      rdw(j, b, 1);
      if (j < 4) rdx(1, a1, j);
      __builtin_amdgcn_sched_barrier(0);
    }
    mmj(1, 0);
    __builtin_amdgcn_sched_barrier(0);
    asm volatile("s_cmp_lg_u32 %0, 0\n\ts_cbranch_scc1 1f\n\ts_waitcnt lgkmcnt(0)\n\ts_barrier\n1:" :: "s"(skip) : "scc", "memory");
    __builtin_amdgcn_sched_barrier(0);
#pragma unroll
    for (int j = 0; j < NT; ++j) {
      if (j > 0) mmj(1, j);
      __builtin_amdgcn_sched_barrier(0);
      rdw(j, b ^ 1, 0);
      if (j < 4) rdx(0, a0, j);
      __builtin_amdgcn_sched_barrier(0);
    }
    xcur = xnext;
  };
#pragma unroll 1
  for (int t = 0; t < 10; t += 2) {
    half(0, 0);
    half(1, t & 8);
  }
  lds_inc(smem + C_DONE1);
  STAMP_AT(1, 3);
  if (ROLE == 0) STAMP_AT(3, 0);
  if (ROLE == 2) STAMP_AT(3, 3);
  lane = lane_id();

  if (ROLE == 0) {
    const unsigned so = AF_OFF + ((rt * 4 + tg) * NS2 + ct * 3) * 4096;
    u32x2 mid[4];
#pragma unroll
    for (int i = 0; i < 4; ++i) {
      unsigned hp[3][2];
#pragma unroll
      for (int u = 0; u < 3; ++u) {
#pragma unroll
        for (int r = 0; r < 2; ++r) {
          const f32x2 gv = {acc[i][2 * u][2 * r], acc[i][2 * u][2 * r + 1]}, uv = {acc[i][2 * u + 1][2 * r], acc[i][2 * u + 1][2 * r + 1]};
          const float c1 = -2.f * 0.7978845608f * 1.4426950409f, c2 = c1 * 0.044715f;
          const f32x2 p = gv * __builtin_elementwise_fma(gv * gv, f32x2{c2, c2}, f32x2{c1, c1});
          const f32x2 d = f32x2{__builtin_amdgcn_exp2f(p[0]), __builtin_amdgcn_exp2f(p[1])} + f32x2{1.f, 1.f};
          const f32x2 h = (gv * uv) * f32x2{__builtin_amdgcn_rcpf(d[0]), __builtin_amdgcn_rcpf(d[1])};
          hp[u][r] = pack2(h[0], h[1]);
        }
      }
      const u32x4 full = {hp[0][0], hp[0][1], hp[1][0], hp[1][1]};
      mid[i] = u32x2{hp[2][0], hp[2][1]};
      st16(rws, lane * 16 + i * 1024, so + ch * 8192, full);
      if (i & 1) st16(rws, (lane & 31) * 16 + (lane >> 5) * 1024 + (i - 1) * 1024, so + 4096 + ch * 512, pair_mid(mid[i - 1], mid[i]));
    }
    STAMP_AT(3, 1);
    if (arrive_last(smem + C_EPI, 8) && lane < 32) { post(ws, bid2(rt * 4 + (lane & 3), lane >> 2), SL_H + ct, tag); PSTAMP(0); }
    STAMP_AT(3, 2);
  } else if (ROLE == 1) {
#pragma unroll
    for (int i = 0; i < 4; ++i)
#pragma unroll
      for (int j = 0; j < 5; ++j) { acc[i][j] = phi4(acc[i][j]); asm volatile("" : "+v"(acc[i][j])); }
    unsigned so = AF_OFF + ((rt * 4 + tg) * NS2 + 54 + ct * 5) * 4096 + ch * 12288 + lane * 16;
    unsigned sm = AF_OFF + ((rt * 4 + tg) * NS2 + 56 + ct * 5) * 4096 + ch * 512 + (lane & 31) * 16 + (lane >> 5) * 1024;
    unsigned za = ZBUF + (ch * 4 + (lane >> 4)) * 80;
    asm volatile("" : "+v"(so), "+v"(sm), "+v"(za));
    lds_wait(smem + C_ZK, 1);
    STAMP_AT(2, 5);
    f32x4 z[5];
#pragma unroll
    for (int j = 0; j < 5; ++j) z[j] = *(const f32x4*)(smem + za + j * 16);
    u32x2 mid[4];
#pragma unroll
    for (int i = 0; i < 4; ++i) {
      f32x4 d4 = acc[i][0] * z[0];
#pragma unroll
      for (int j = 1; j < 5; ++j) d4 = __builtin_elementwise_fma(acc[i][j], z[j], d4);
      const float inv = __builtin_amdgcn_rcpf(((d4[0] + d4[1]) + (d4[2] + d4[3])) + 1e-6f);
      u32x2 a[5];
#pragma unroll
      for (int j = 0; j < 5; ++j) a[j] = pack4(acc[i][j] * inv);
      st16(rws, so, i * 1024, u32x4{a[0][0], a[0][1], a[1][0], a[1][1]});
      st16(rws, so, i * 1024 + 4096, u32x4{a[2][0], a[2][1], a[3][0], a[3][1]});
      mid[i] = a[4];
      if (i & 1) st16(rws, sm, (i - 1) * 1024, pair_mid(mid[i - 1], mid[i]));
    }
    if (wid == 7) PSTAMP(4);
    lds_inc(smem + C_EPI);
    lds_wait(smem + C_EPI, 8);
  } else {
    bf16x8 op[2][5];
    const unsigned isv = bitmask(lane, 3);
#pragma unroll
    for (int hh = 0; hh < 2; ++hh)
#pragma unroll
      for (int j = 0; j < 5; ++j) {
        f32x4 a = acc[2 * hh][j], b = acc[2 * hh + 1][j];
        u32x2 pa, pb;
        if (j == 2 || j == 3) { pa = pack4(a); pb = pack4(b); }
        else {
          pa = pack4(phi4(a)); pb = pack4(phi4(b));
          if (j == 4) {
            const u32x2 ra = pack4(a), rb = pack4(b);
            pa = u32x2{sel(isv, ra[0], pa[0]), sel(isv, ra[1], pa[1])}; pb = u32x2{sel(isv, rb[0], pb[0]), sel(isv, rb[1], pb[1])};
          }
        }
        op[hh][j] = __builtin_bit_cast(bf16x8, u32x4{pa[0], pa[1], pb[0], pb[1]});
      }
    f32x4 y[7];
#pragma unroll
    for (int n = 0; n < 7; ++n) y[n] = f32x4{0.f, 0.f, 0.f, 0.f};
#pragma unroll
    for (int hh = 0; hh < 2; ++hh) {
      const u32x4 t4 = __builtin_bit_cast(u32x4, op[hh][4]);
      u32x4 x4, r4;
#pragma unroll
      for (int i = 0; i < 4; ++i) {
        x4[i] = sel(isv, t4[i], 0x3f803f80u);
        r4[i] = (unsigned)__builtin_amdgcn_update_dpp(0, (int)t4[i], 0x128, 0xf, 0xf, true);
      }
      const bf16x8 X4 = __builtin_bit_cast(bf16x8, x4), R4 = __builtin_bit_cast(bf16x8, r4);
      y[0] = mfma16(op[hh][2], op[hh][0], y[0]);
      y[1] = mfma16(op[hh][3], op[hh][1], y[1]);
      y[2] = mfma16(X4, op[hh][0], y[2]);
      y[3] = mfma16(X4, op[hh][1], y[3]);
      y[4] = mfma16(op[hh][2], op[hh][4], y[4]);
      y[5] = mfma16(op[hh][3], op[hh][4], y[5]);
      y[6] = mfma16(X4, R4, y[6]);
    }
    f32x4* red = (f32x4*)(smem + 40960);
    f32x4* mine = red + (tg * 8 + ch * 4) * 64 + lane;
    mine[0] = y[0];
    mine[64] = y[1];
    mine[128] = sel4(bitmask(lane, 4), y[3], y[2]);
    mine[192] = sel4(isv, y[6], sel4(bitmask(lane, 2), y[5], y[4]));
    lds_inc(smem + C_KVB);
    STAMP_AT(3, 5);
    const int fr2 = lane & 15, g2 = lane >> 4, q2 = fr2 >> 2, p2 = lane & 3, tile = wid & 3;
    unsigned kpa = BOX_OFF + bid1(lane & 7, (lane & 8) ? 22 + ct : 18 + (ct >> 1)) * 2048 + (SL_KV + ((lane & 8) ? 0 : (ct & 1) * 8) + rt) * 8;
    asm volatile("" : "+v"(kpa));
    lds_wait(smem + C_KVB, 8);
    const f32x4* src = red + wid * 64 + lane;
    const f32x4 s = (src[0] + src[512]) + (src[1024] + src[1536]);
    __builtin_amdgcn_raw_buffer_store_b64(pack4(s), rws, (wid * 64 + lane) * 8 + KVP_OFF + (rt * 8 + ct) * 4096, 0, 16);
    if (tile >= 2 && (tile == 2 ? g2 < 2 : (g2 == 0 && fr2 >= 8)))
      __builtin_amdgcn_raw_buffer_store_b32(__builtin_bit_cast(unsigned, s[0]), rws,
                                            ZP_OFF + (rt * 8 + ct) * 384 + ch * 160 + (tile == 2 ? g2 * 20 + fr2 : (q2 & 1) * 20 + 16 + p2) * 4, 0, 16);
    *(u32x2*)(smem + KVI + (wid * 64 + lane) * 8) = u32x2{0u, 0u};
    lds_inc(smem + C_KVD);
    STAMP_AT(3, 6);
    if (arrive_last(smem + C_EPI, 8) && lane < 16) { __hip_atomic_store((GAS u64*)(ws + kpa), tag, RLX_AGENT); PSTAMP(2); }
    STAMP_AT(3, 7);
    lds_wait(smem + C_WO, 4);
    bf16x8 bw[3];
#pragma unroll
    for (int m = 0; m < 3; ++m) {
      const char* pw = smem + WOB + (g2 * 20 + m * 8 + q2) * 160 + (wid < 5 ? wid : 0) * 32 + p2 * 8;
      bw[m] = cat8(tr_read(pw), tr_read(pw + (m < 2 ? 4 * 160 : 0)));
    }
    int h = 2 * ch, d, e = 4 * g2;
    bool ok = true;
    if (tile < 2) { h += tile; d = fr2; }
    else if (tile == 2) { h += g2 & 1; d = fr2; e = 16; ok = g2 >= 2; }
    else { h += q2 & 1; d = 16 + p2; if (fr2 >= 8) { e = 16; ok = g2 == 2 + (q2 & 1); } }
    int dst = ok ? (h * 20 + d) * 48 + e * 2 : 81 * 48;
    int aoff = q2 == g2 ? (g2 * 20 + p2) * 48 : 80 * 48, astep = q2 == g2 ? 4 * 48 : 0;
    asm volatile("" : "+v"(dst), "+v"(aoff), "+v"(astep));
    if (wid == 0) { await<1>(ws + BOX_OFF + blockIdx.x * 2048 + SL_KV * 8, 8, tag, lane); lds_st(smem + C_QK, 1); PSTAMP(6); } else lds_wait(smem + C_QK, 1);
    u32x2 pp[8];
#pragma unroll
    for (int k = 0; k < 8; ++k) pp[k] = __builtin_amdgcn_raw_buffer_load_b64(rws, (wid * 64 + lane) * 8, KVP_OFF + (k * 8 + ct) * 4096, 16);
    f32x4 t = {0.f, 0.f, 0.f, 0.f};
#pragma unroll
    for (int k = 0; k < 8; ++k) t += f32x4{bf_lo(pp[k][0]), bf_hi(pp[k][0]), bf_lo(pp[k][1]), bf_hi(pp[k][1])};
    *(u32x2*)(smem + KVI + dst) = pack4(t);
    lds_inc(smem + C_MB);
    if (wid < 5) {
      lds_wait(smem + C_MB, 8);
      u32x2 a[5];
#pragma unroll
      for (int j = 0; j < 5; ++j) {
        f32x4 c = {0.f, 0.f, 0.f, 0.f};
#pragma unroll
        for (int m = 0; m < 3; ++m) c = mfma16(*(const bf16x8*)(smem + KVI + aoff + j * astep + m * 16), bw[m], c);
        a[j] = pack4(c);
      }
      const unsigned to = WF_OFF + (rt * NS2 + 54 + (ct >> 1) * 5) * 5120 + wid * 1024;
      st16(rws, lane * 16, to + (ct & 1) * 15360, u32x4{a[0][0], a[0][1], a[1][0], a[1][1]});
      st16(rws, lane * 16, to + (ct & 1) * 15360 + 5120, u32x4{a[2][0], a[2][1], a[3][0], a[3][1]});
      const u32x4 md = pair_mid(a[4], a[4]);
      if (lane < 32) st16(rws, lane * 16, to + 10240 + (ct & 1) * 512, md);
    }
  }
}

DEVI void spare_loader(char* smem, const char* __restrict__ wd, int id, int lw, int lane) {
  const int n = id < 6 ? 4 : 3;
  const rsrc_t rs = mk_rsrc(wd, F_ * 1280);
  auto ld = [&](int k) {
    const int s = id + k * 16;
    char* dst = smem + (k % 3) * 40960 + lw * 10240;
#pragma unroll
    for (int h = 0; h < 2; ++h) {
      const int row0 = s % 3 == 1 ? s * 32 + 16 * (lw >> 1) + 4 * (lw & 1) + 8 * h : s * 32 + 4 * lw + 16 * h;
#pragma unroll
      for (int i = 0; i < 5; ++i) dma16<0>(rs, dst + (h * 5 + i) * 1024, lane * 16, row0 * 1280 + i * 1024);
    }
  };
  ld(0); ld(1); ld(2);
#pragma unroll 1
  for (int k = 0; k < n; ++k) {
    if (k == 0) asm volatile("s_waitcnt vmcnt(20)" ::: "memory");
    else if (k + 1 < n) asm volatile("s_waitcnt vmcnt(10)" ::: "memory");
    else drain();
    bar();
    if (k == 1 && n == 4) ld(3);
  }
  bar();
}
DEVI void spare_compute(char* smem, rsrc_t rws, char* ws, int id, int wid, int lane, u64 tag) {
  const int fr = lane & 15, g = lane >> 4, q = fr >> 2, p = lane & 3;
  const int n = id < 6 ? 4 : 3;
#pragma unroll 1
  for (int k = 0; k < n; ++k) {
    bar();
    const int s = id + k * 16;
#pragma unroll
    for (int j = 0; j < 5; ++j) {
      const char* pp = smem + (k % 3) * 40960 + (g * 8 + q) * 1280 + (wid * 5 + j) * 32 + p * 8;
      const bf16x8 v = cat8(tr_read(pp), tr_read(pp + 4 * 1280));
      st16(rws, lane * 16 + j * 1024, WF_OFF + (wid * NS2 + s) * 5120, __builtin_bit_cast(u32x4, v));
    }
    asm volatile("s_waitcnt lgkmcnt(0)" ::: "memory");
  }
  drain();
  bar();
  if (wid < 4) post(ws, wid * 64 + lane, SL_RL + id, tag);
  STAMP_AT(1, 2);
}

constexpr unsigned CODE_BYTES = 36000;
#define THR 5
constexpr int NST = 7;
#define RD 4
#define NG 10
#define MW 10
DEVI int g2_step(int gI, int kq) { return gI < 13 ? 4 * gI + kq : gI == 13 ? 52 + (kq & 1) : 54 + 5 * kq + (gI - 14); }
__global__ __launch_bounds__(768, 3) void fused(const char* __restrict__ x, const char* __restrict__ wqkv, const char* __restrict__ wo,
                                                const char* __restrict__ wg, const char* __restrict__ wu, const char* __restrict__ wd,
                                                char* __restrict__ out, char* ws) {
  extern __shared__ __attribute__((aligned(16))) char smem[];
  const u64 pt0 = __builtin_amdgcn_s_memrealtime();
  u64 pt1 = pt0;
  int lane = threadIdx.x & 63;
  const int wid = __builtin_amdgcn_readfirstlane(threadIdx.x >> 6);
  STAMP_AT(1, 0);
  STAMP_AT(0, 0);
  if (wid == 8) {
    u64 pc;
    asm volatile("s_getpc_b64 %0" : "=s"(pc));
    const unsigned off = ((blockIdx.x >> 3) * 16 + (lane & 15)) * 128;
    __builtin_amdgcn_global_load_lds((const GAS void*)((pc & ~127ull) + (off < CODE_BYTES ? off : 0u)), (LAS void*)(smem + MISC), 4, 0, 0);
  }
  if (threadIdx.x < 28) *(unsigned*)(smem + CNT + threadIdx.x * 4) = 0u;
  const int bid = blockIdx.x, xcd = bid & 7, idx = bid >> 3;
  const int cs = idx >> 2, xb = xcd >> 1;
  const int rt = (xcd & 1) * 4 + (cs < 7 ? idx & 3 : (xb & 1) * 2 + (idx & 1));
  const int ct = cs < 4 ? xb * 4 + cs : cs == 4 ? 18 + xb : cs < 7 ? 17 + xb * 2 + cs : (idx & 2) ? 30 : 16 + (xb >> 1);
  const int sid = xcd * 2 + (idx & 1);
  const int r2 = (xcd >> 1) * 8 + (idx >> 2), c2 = (xcd & 1) * 4 + (idx & 3);
  const rsrc_t rws = mk_rsrc(ws, WS_END);
  const char* box = ws + BOX_OFF + bid * 2048;

  if (__builtin_expect(wid < 8, 1)) {
    const int row = bid * 8 + wid;
    const u64 gen = __hip_atomic_load((const GAS u64*)(ws + GEN_OFF), RLX_AGENT);
    const u32x4 v0 = *(const u32x4*)(x + row * 1280 + lane * 16);
    const u32x4 v1 = *(const u32x4*)(x + row * 1280 + 1024 + (lane & 15) * 16);
    float f[16];
#pragma unroll
    for (int i = 0; i < 4; ++i) { f[2 * i] = bf_lo(v0[i]); f[2 * i + 1] = bf_hi(v0[i]); f[8 + 2 * i] = bf_lo(v1[i]); f[9 + 2 * i] = bf_hi(v1[i]); }
    if (PROBE == 4) { asm volatile("s_waitcnt vmcnt(0)" ::: "memory"); pt1 = __builtin_amdgcn_s_memrealtime(); }
    const float sh = rdl(f[0], 0);
    float s0 = 0.f, q0 = 0.f, s1 = 0.f, q1 = 0.f;
#pragma unroll
    for (int i = 0; i < 8; ++i) {
      f[i] -= sh; f[8 + i] -= sh;
      s0 += f[i]; q0 += f[i] * f[i];
      s1 += f[8 + i]; q1 += f[8 + i] * f[8 + i];
    }
    if (lane < 16) { s0 += s1; q0 += q1; }
    const float m = wave_sum(s0) * (1.f / 640.f);
    const float var = fmaxf(wave_sum(q0) * (1.f / 640.f) - m * m, 0.f);
    const float rs = rsqrtf(var + 1e-5f);
    u32x4 o0, o1;
#pragma unroll
    for (int i = 0; i < 4; ++i) {
      o0[i] = pack2((f[2 * i] - m) * rs, (f[2 * i + 1] - m) * rs);
      o1[i] = pack2((f[8 + 2 * i] - m) * rs, (f[9 + 2 * i] - m) * rs);
    }
    const int rr = row & 255;
    const unsigned vo = (lane >> 3) * 32768 + (((lane & 7) ^ ((rr >> 1) & 7)) << 4), so = XN_OFF + (row >> 8) * 327680 + rr * 128;
    st16(rws, vo, so, o0);
    if (lane < 16) st16(rws, vo + 8 * 32768, so, o1);
    STAMP_AT(2, 1);
    const u64 tag = uni64(gen) + 1;
    if (threadIdx.x == 0) *(u64*)(smem + C_TAG) = tag;
    unsigned pa = lane < 30 ? BOX_OFF + bid1(bid >> 5, lane) * 2048 + (SL_XN + (bid & 31)) * 8 : ALL_OFF + bid * 8;
    asm volatile("" : "+v"(pa));
    asm volatile("s_waitcnt vmcnt(0) lgkmcnt(0)" ::: "memory");
    bar();
    if (wid == 0 && lane < 31) __hip_atomic_store((GAS u64*)(ws + pa), tag, RLX_AGENT);
    if (PROBE == 1) pt1 = __builtin_amdgcn_s_memrealtime();
    if (ct < 18) g1_compute<0>(smem, rws, ws, rt, ct, wid, lane, tag);
    else if (ct < 22) g1_compute<1>(smem, rws, ws, rt, ct - 18, wid, lane, tag);
    else if (ct < 30) g1_compute<2>(smem, rws, ws, rt, ct - 22, wid, lane, tag);
    else spare_compute(smem, rws, ws, sid, wid, lane, tag);
    STAMP_AT(1, 4);

    lane = lane_id();
    const int kq = wid & 3, th = wid >> 2;
    f32x4 acc[2][5];
#pragma unroll
    for (int i = 0; i < 2; ++i)
#pragma unroll
      for (int j = 0; j < 5; ++j) acc[i][j] = f32x4{0.f, 0.f, 0.f, 0.f};
    u32x4 af[RD][2];
    bf16x8 wf[5];
    const unsigned aso = AF_OFF + r2 * NS2 * 4096 + th * 2048;
    auto lda = [&](int gI) {
      const unsigned so = aso + g2_step(gI, kq) * 4096;
      af[gI % RD][0] = ld16(rws, lane * 16, so);
      af[gI % RD][1] = ld16(rws, lane * 16 + 1024, so);
    };
    unsigned wl[3];
    auto rdw = [&](int gI, int j) { wf[j] = *(const LAS bf16x8*)(size_t)(wl[(gI % 7) / 3] + ((gI % 7) % 3) * 20480 + j * 1024); };
    const bool isq = ct >= 18 && ct < 22;
    bool pend = isq || (ct >= 22 && ct < 30 && wid < 5);
    auto announce = [&](bool last) {
      if (last && lane < 32) { post(ws, isq ? bid2(rt * 4 + (lane & 3), lane >> 2) : bid2(lane, rt), isq ? SL_O + ct - 18 : SL_M + ct - 22, tag); PSTAMP(isq ? 1 : 3); }
    };
    if (pend && __builtin_amdgcn_readfirstlane(lds_ld_now(smem + C_H)) == 0) { announce(arrive_last(smem + C_M, isq ? 8 : 5)); pend = false; }
    lds_wait(smem + C_H, 1);
    if (PROBE == 3 && ct < 18) pt1 = __builtin_amdgcn_s_memrealtime();
#pragma unroll
    for (int gI = 0; gI < RD; ++gI) lda(gI);
    __builtin_amdgcn_sched_barrier(0);
    if (pend) announce(arrive_last<2 * RD>(smem + C_M, isq ? 8 : 5));
#pragma unroll
    for (int k = 0; k < 3; ++k) { wl[k] = (unsigned)(size_t)(LAS char*)smem + kq * 5120 + lane * 16 + k * 61440; asm volatile("" : "+v"(wl[k])); }
    lds_wait(smem + C_FULL + kq * 4, 7);
#pragma unroll
    for (int j = 0; j < 5; ++j) rdw(0, j);
    __builtin_amdgcn_sched_barrier(0);
    unsigned full = 7;
    u64 oflag = 0;
#pragma unroll
    for (int gI = 0; gI < 19; ++gI) {
      if (gI + 1 >= 7 && gI + 1 < 19) {
        if ((unsigned)__builtin_amdgcn_readfirstlane(full) < (unsigned)(gI + 2)) lds_wait(smem + C_FULL + kq * 4, gI + 2);
      }
      if (gI == 13 && kq >= 2) { af[gI % RD][0] = u32x4{0u, 0u, 0u, 0u}; af[gI % RD][1] = u32x4{0u, 0u, 0u, 0u}; }
#pragma unroll
      for (int j = 0; j < 5; ++j) {
        acc[0][j] = mfma16(wf[j], __builtin_bit_cast(bf16x8, af[gI % RD][0]), acc[0][j]);
        acc[1][j] = mfma16(wf[j], __builtin_bit_cast(bf16x8, af[gI % RD][1]), acc[1][j]);
        __builtin_amdgcn_sched_barrier(0);
        if (gI + 1 < 19) rdw(gI + 1, j);
        if (j == 2 && gI + 2 >= 7 && gI + 2 < 19) full = lds_ld(smem + C_FULL + kq * 4);
        __builtin_amdgcn_sched_barrier(0);
      }
      if (gI + 1 < 12) { asm volatile("" ::: "memory"); lds_st(smem + C_FREE + kq * 8 + th * 4, gI + 2); }
      if (gI == 14 - RD - 3) oflag = __hip_atomic_load((const GAS u64*)(box + SL_O * 8) + kq, RLX_AGENT);
      if (gI == 14 - RD) {
        if (!__all(oflag == tag)) await<2>(box + (SL_O + kq) * 8, 1, tag, lane);
        __builtin_amdgcn_fence(__ATOMIC_ACQUIRE, "wavefront");
        STAMP_AT(2, 6);
      }
      if (gI + RD < 19) lda(gI + RD);
      __builtin_amdgcn_sched_barrier(0);
    }
    STAMP_AT(2, 2);
    asm volatile("s_waitcnt lgkmcnt(0)" ::: "memory");
    bar();
    STAMP_AT(2, 3);
    {
      lane = lane_id();
      char* red = smem + kq * 21504 + (th * 32 + (lane & 15)) * 336 + (lane >> 4) * 16;
#pragma unroll
      for (int i = 0; i < 2; ++i)
#pragma unroll
        for (int j = 0; j < 5; ++j) *(f32x4*)(red + i * 16 * 336 + j * 64) = acc[i][j];
    }
  } else {
    const int lw = wid - 8;
    if (ct < 30) {
      const int q = lane & 3, r = (rt & 3) * 16 + (lane >> 2);
      const unsigned tstr = ct < 18 ? F_ * 2 : 1920 * 2;
      const char* base = ct < 18 ? ((q & 2) ? wu : wg) : wqkv;
      const int col = ct < 18 ? ct * 192 + (q & 1) * 128 : ct < 22 ? (ct - 18) * 320 + (q < 3 ? q * 128 : 316) : 1280 * (1 + (q >> 1)) + (ct - 22) * 160 + (q & 1) * 128;
      const char* tp = base + r * tstr + col;
      auto touch = [&](int t) { __builtin_amdgcn_global_load_lds((const GAS void*)(tp + (size_t)(t * 64 * tstr)), (LAS void*)(smem + MISC), 4, 0, 0); };
      touch(lw);
      g1_loader(smem, rws, ct < 18 ? (lw >= 2 ? wu : wg) : wqkv, ws, rt, ct, lw, lane, touch);
    } else {
      bar();
      __builtin_amdgcn_s_sleep(2);
      spare_loader(smem, wd, sid, lw, lane);
    }
    const u64 tag = uni64(*(const u64*)(smem + C_TAG));
    const unsigned wso = WF_OFF + c2 * NS2 * 5120;
    auto ldw = [&](int gI) {
#pragma unroll
      for (int j = 0; j < 5; ++j) dma16<16>(rws, smem + ((gI % 7) * 4 + lw) * 5120 + j * 1024, lane * 16, wso + g2_step(gI, lw) * 5120 + j * 1024);
    };
    u32x4 wr[NST][5];
    auto ldr = [&](int gI) {
#pragma unroll
      for (int j = 0; j < 5; ++j) wr[gI - 7][j] = ld16(rws, lane * 16 + j * 1024, wso + g2_step(gI, lw) * 5120);
    };
    auto thr = [&]() { __builtin_amdgcn_sched_barrier(0); asm volatile("s_waitcnt vmcnt(%0)" :: "n"(THR) : "memory"); __builtin_amdgcn_sched_barrier(0); };
    if (bid == 255 && lw == 0) {
#pragma unroll 1
      for (int k = 0; k < 4; ++k) await<4>(ws + ALL_OFF + k * 512, 64, tag, lane);
      if (lane == 0) __hip_atomic_store((GAS u64*)(ws + GEN_OFF), tag, RLX_AGENT);
    }
    await<PSL>(box + SL_RL * 8, 16, tag, lane);
    drain();
    if (PROBE == 2 && ct < 18) pt1 = __builtin_amdgcn_s_memrealtime();
    ldr(7); ldr(8);
    __builtin_amdgcn_sched_barrier(0);
    if (ct < 30) bar();
    if (ct >= 22 && ct < 30) {
      const rsrc_t ro = mk_rsrc(wo, D_ * 1280);
#pragma unroll 1
      for (int i = lw; i < 13; i += 4) {
        const int ci = i * 64 + lane, cc = ci < 800 ? ci : 799;
        dma16<0>(ro, smem + WOB + i * 1024, (cc / 10) * 1280 + (cc % 10) * 16, (ct - 22) * 80 * 1280 + rt * 160);
      }
    }
    ldw(4); thr(); ldw(5); thr();
    if (ct >= 22 && ct < 30 && lane == 0) asm volatile("ds_add_u32 %0, %1" :: "v"((unsigned)(size_t)(LAS char*)(smem + C_WO)), "v"(1u) : "memory");
    if (ct < 22 || ct >= 30) { ldw(2); thr(); ldw(3); thr(); }
    ldr(9); thr(); ldr(10); thr();
    if (ct < 30) lds_wait(smem + C_DONE1, 8);
    ldw(0); thr(); ldw(1); thr(); ldw(6); thr();
    if (ct >= 22 && ct < 30) { lds_wait(smem + C_KVD, 8); ldw(2); thr(); ldw(3); thr(); }
    STAMP_AT(0, 3);
    ldr(11);
    __builtin_amdgcn_sched_barrier(0);
    asm volatile("s_waitcnt vmcnt(5)" ::: "memory");
    lds_st(smem + C_FULL + lw * 4, 7);
    ldr(12); thr();
    if (ct >= 18 && ct < 22 && lw == 0) {
      await<1>(box + SL_KV * 8, 16, tag, lane);
      PSTAMP(5);
      const int l = lane < 40 ? lane : 39;
      u32x4 zp[8];
#pragma unroll
      for (int k = 0; k < 8; ++k) zp[k] = ld16(rws, l * 16 + (l >= 20 ? 64 : 0), ZP_OFF + (k * 8 + (ct - 18) * 2) * 384);
      f32x4 zs = __builtin_bit_cast(f32x4, zp[0]);
#pragma unroll
      for (int k = 1; k < 8; ++k) zs += __builtin_bit_cast(f32x4, zp[k]);
      *(f32x4*)(smem + ZBUF + l * 16) = zs;
      lds_st(smem + C_ZK, 1);
    }
    __builtin_amdgcn_sched_barrier(0);
    lane = lane_id();
    ldr(13);
    __builtin_amdgcn_sched_barrier(0);
    STAMP_AT(0, 4);
    if (lw == 1) {
      if (ct < 18) lds_wait(smem + C_EPI, 8);
      else if (ct < 22) lds_wait(smem + C_ZK, 1);
      else if (ct < 30) lds_wait(smem + C_MB, 8);
      await<PSL>(box + SL_H * 8, 18, tag, lane);
      lds_st(smem + C_H, 1);
      HSTAMP;
    }
    const char* mbox = box + (SL_M + lw * 2) * 8;
    char* wdst = smem + lw * 5120 + lane * 16;
    auto wait_free = [&](unsigned n) {
#pragma unroll 1
      while (!__all(lds_ld_now(smem + C_FREE + lw * 8 + (lane & 1) * 4) >= n)) __builtin_amdgcn_s_sleep(1);
      asm volatile("" ::: "memory");
    };
    u64 mflag = 0;
#pragma unroll
    for (int gI = 7; gI < 14; ++gI) {
      wait_free(gI - 6);
#pragma unroll
      for (int j = 0; j < 5; ++j) *(u32x4*)(wdst + (gI % 7) * 20480 + j * 1024) = wr[gI - 7][j];
      asm volatile("" ::: "memory");
      lds_st(smem + C_FULL + lw * 4, gI + 1);
      if (gI == MW) mflag = __hip_atomic_load((const GAS u64*)mbox + (lane & 1), RLX_AGENT);
      __builtin_amdgcn_sched_barrier(0);
    }
    if (!__all(mflag == tag)) await<2>(mbox, 2, tag, lane);
    if (lw == 0) PSTAMP(7);
    __builtin_amdgcn_fence(__ATOMIC_ACQUIRE, "wavefront");
#pragma unroll
    for (int gI = 14; gI < 19; ++gI) {
      wait_free(gI - 6);
      ldw(gI);
      if (gI >= 16) { asm volatile("s_waitcnt vmcnt(10)" ::: "memory"); lds_st(smem + C_FULL + lw * 4, gI - 1); }
    }
    asm volatile("s_waitcnt vmcnt(5)" ::: "memory");
    lds_st(smem + C_FULL + lw * 4, 18);
    drain();
    lds_st(smem + C_FULL + lw * 4, 19);
    STAMP_AT(0, 6);
    bar();
  }
  const int tid = wid * 64 + lane_id();
  const int tok = tid / 10, m8 = tid % 10;
  const unsigned oo = (r2 * 64 + tok) * 1280 + c2 * 160 + m8 * 16;
  u32x4 xr = {0u, 0u, 0u, 0u};
  if (tid < 640) xr = *(const u32x4*)(x + oo);
  asm volatile("s_waitcnt lgkmcnt(0)" ::: "memory");
  bar();
  STAMP_AT(2, 4);
  if (tid < 640) {
    const char* rp = smem + tok * 336 + m8 * 32;
    f32x4 lo = *(const f32x4*)rp, hi = *(const f32x4*)(rp + 16);
#pragma unroll
    for (int k = 1; k < 4; ++k) { lo += *(const f32x4*)(rp + k * 21504); hi += *(const f32x4*)(rp + k * 21504 + 16); }
    *(u32x4*)(out + oo) = u32x4{pack2(lo[0] + bf_lo(xr[0]), lo[1] + bf_hi(xr[0])), pack2(lo[2] + bf_lo(xr[1]), lo[3] + bf_hi(xr[1])),
                                pack2(hi[0] + bf_lo(xr[2]), hi[1] + bf_hi(xr[2])), pack2(hi[2] + bf_lo(xr[3]), hi[3] + bf_hi(xr[3]))};
  }
  if (PROBE == 5) pt1 = __builtin_amdgcn_s_memrealtime();
  { const u64 until = pt0 + 6000 + 2 * (pt1 - pt0); while (__builtin_amdgcn_s_memrealtime() < until) __builtin_amdgcn_s_sleep(4); }
}

extern "C" void kernel_launch(void* const* d_in, const int* in_sizes, int n_in, void* d_out, int out_size, void* d_ws,
                              size_t ws_size, hipStream_t stream) {
  (void)hipFuncSetAttribute((const void*)fused, hipFuncAttributeMaxDynamicSharedMemorySize, LDS_BYTES);
  fused<<<256, 768, LDS_BYTES, stream>>>((const char*)d_in[0], (const char*)d_in[1], (const char*)d_in[2], (const char*)d_in[4],
                                         (const char*)d_in[5], (const char*)d_in[6], (char*)d_out, (char*)d_ws);
}
```

```cpp
#include <hip/hip_runtime.h>
#include <hip/hip_bf16.h>
#include <stdint.h>

typedef __attribute__((ext_vector_type(8))) __bf16 bf16x8;
typedef __attribute__((ext_vector_type(2))) __bf16 bf16x2;
typedef __attribute__((ext_vector_type(4))) short s16x4;
typedef __attribute__((ext_vector_type(4))) float f32x4;
typedef __attribute__((ext_vector_type(2))) float f32x2;
typedef __attribute__((ext_vector_type(4))) unsigned u32x4;
typedef __attribute__((ext_vector_type(2))) unsigned u32x2;
typedef unsigned long long u64;
#define LAS __attribute__((address_space(3)))
#define GAS __attribute__((address_space(1)))
#define DEVI __device__ __forceinline__
#define RLX_AGENT __ATOMIC_RELAXED, __HIP_MEMORY_SCOPE_AGENT
typedef __amdgpu_buffer_rsrc_t rsrc_t;

constexpr int S_ = 2048, D_ = 640, F_ = 1728;
constexpr int NS2 = 74;
constexpr unsigned GEN_OFF = 0;
constexpr unsigned ST_OFF = 8192;
constexpr unsigned CS_OFF = ST_OFF + 2048 * 16;
constexpr unsigned BOX_OFF = CS_OFF + 30 * 8 * 1536;
constexpr unsigned AF_OFF = BOX_OFF + 256 * 1024;
constexpr unsigned WF_OFF = AF_OFF + 32 * NS2 * 4096;
constexpr unsigned KVP_OFF = WF_OFF + 8 * NS2 * 5120;
constexpr unsigned ZP_OFF = KVP_OFF + 64 * 8192;
constexpr unsigned WS_END = ZP_OFF + 32 * 8192;
static_assert(AF_OFF % 128 == 0 && BOX_OFF % 128 == 0, "align");
constexpr int SL_H = 0, SL_O = 36, SL_M = 40, SL_RL = 48, SL_H1 = 64;

DEVI float as_f(unsigned v) { return __builtin_bit_cast(float, v); }
DEVI float bf_lo(unsigned v) { return __builtin_bit_cast(float, v << 16); }
DEVI float bf_hi(unsigned v) { return __builtin_bit_cast(float, v & 0xffff0000u); }
DEVI unsigned pack2(float a, float b) {
  f32x2 f = {a, b};
  return __builtin_bit_cast(unsigned, __builtin_convertvector(f, bf16x2));
}
DEVI rsrc_t mk_rsrc(const void* p, unsigned bytes) { return __builtin_amdgcn_make_buffer_rsrc((void*)p, 0, bytes, 0x00020000); }
DEVI f32x4 mfma16(bf16x8 a, bf16x8 b, f32x4 c) { return __builtin_amdgcn_mfma_f32_16x16x32_bf16(a, b, c, 0, 0, 0); }
DEVI s16x4 tr_read(const char* p) { return __builtin_amdgcn_ds_read_tr16_b64_v4i16((LAS s16x4*)p); }
DEVI bf16x8 cat8(s16x4 a, s16x4 b) {
  typedef __attribute__((ext_vector_type(8))) short s16x8;
  s16x8 r = __builtin_shufflevector(a, b, 0, 1, 2, 3, 4, 5, 6, 7);
  return __builtin_bit_cast(bf16x8, r);
}
DEVI float exp2_le1(float t) { return __builtin_amdgcn_fmed3f(__builtin_amdgcn_exp2f(t), 0.f, 1.f); }
DEVI f32x2 phi2(f32x2 t) {
  const f32x2 s = t * f32x2{1.4426950409f, 1.4426950409f};
  const f32x2 u = {__builtin_elementwise_maximum(t[0], 1e-6f), __builtin_elementwise_maximum(t[1], 1e-6f)};
  return u + f32x2{exp2_le1(s[0]), exp2_le1(s[1])};
}
DEVI unsigned sel(unsigned m, unsigned a, unsigned b) { return (m & a) | (~m & b); }
DEVI f32x4 sel4(unsigned m, f32x4 a, f32x4 b) {
  const u32x4 x = __builtin_bit_cast(u32x4, a), y = __builtin_bit_cast(u32x4, b);
  return __builtin_bit_cast(f32x4, u32x4{sel(m, x[0], y[0]), sel(m, x[1], y[1]), sel(m, x[2], y[2]), sel(m, x[3], y[3])});
}
DEVI unsigned bitmask(int lane, int bit) { unsigned m = (unsigned)(((int)((unsigned)lane << (31 - bit))) >> 31); asm volatile("" : "+v"(m)); return m; }
DEVI f32x4 phi4(f32x4 t) {
  const f32x2 a = phi2(f32x2{t[0], t[1]}), b = phi2(f32x2{t[2], t[3]});
  return f32x4{a[0], a[1], b[0], b[1]};
}
DEVI f32x4 splat4(float v) { return f32x4{v, v, v, v}; }
DEVI u32x2 pack4(f32x4 v) { return u32x2{pack2(v[0], v[1]), pack2(v[2], v[3])}; }
template <int AUX> DEVI void dma16(rsrc_t rs, char* lds, unsigned voff, unsigned soff) {
  __builtin_amdgcn_raw_ptr_buffer_load_lds(rs, (LAS void*)lds, 16, voff, soff, 0, AUX);
}
DEVI void st16(rsrc_t rs, unsigned voff, unsigned soff, u32x4 v) { __builtin_amdgcn_raw_buffer_store_b128(v, rs, voff + soff, 0, 16); }
DEVI u32x4 ld16(rsrc_t rs, unsigned voff, unsigned soff) { return __builtin_amdgcn_raw_buffer_load_b128(rs, voff, soff, 16); }
DEVI void drain() { asm volatile("s_waitcnt vmcnt(0)" ::: "memory"); }
DEVI void bar() { __builtin_amdgcn_sched_barrier(0); asm volatile("s_barrier" ::: "memory"); __builtin_amdgcn_sched_barrier(0); }
DEVI void post(char* ws, int consumer, int slot, u64 tag) {
  __hip_atomic_store((GAS u64*)(ws + BOX_OFF + consumer * 1024 + slot * 8), tag, RLX_AGENT);
}
template <int SL = 1> DEVI void await(const char* p, int n, u64 tag, int lane) {
  const GAS u64* q = (const GAS u64*)(p) + (lane < n ? lane : 0);
#pragma unroll 1
  for (int spin = 0; spin < (1 << 18); ++spin) {
    u64 v = __hip_atomic_load(q, RLX_AGENT);
    if (__all(v == tag)) break;
    __builtin_amdgcn_s_sleep(SL);
  }
  __builtin_amdgcn_fence(__ATOMIC_ACQUIRE, "wavefront");
}
template <int CTRL> DEVI float dpp_add(float v) {
  return v + __builtin_bit_cast(float, __builtin_amdgcn_update_dpp(0, __builtin_bit_cast(int, v), CTRL, 0xf, 0xf, true));
}
DEVI float rdl(float v, int l) { return __builtin_bit_cast(float, __builtin_amdgcn_readlane(__builtin_bit_cast(int, v), l)); }
DEVI float wave_sum(float v) {
  v = dpp_add<0xB1>(v);
  v = dpp_add<0x4E>(v);
  v = dpp_add<0x141>(v);
  v = dpp_add<0x140>(v);
  return (rdl(v, 0) + rdl(v, 16)) + (rdl(v, 32) + rdl(v, 48));
}
DEVI int lane_id() { int l; asm volatile("v_mbcnt_lo_u32_b32 %0, -1, 0\n\tv_mbcnt_hi_u32_b32 %0, -1, %0" : "=v"(l)); return l; }
template <int KEEP = 0> DEVI bool arrive_last(char* cnt, unsigned n) {
  asm volatile("s_waitcnt vmcnt(%0)" :: "n"(KEEP) : "memory");
  unsigned old = 0;
  if (lane_id() == 0) old = __hip_atomic_fetch_add((LAS unsigned*)cnt, 1u, __ATOMIC_RELAXED, __HIP_MEMORY_SCOPE_WORKGROUP);
  return __builtin_amdgcn_readfirstlane(old) == n - 1;
}
DEVI u64 uni64(u64 v) { return ((u64)(unsigned)__builtin_amdgcn_readfirstlane((unsigned)(v >> 32)) << 32) | (unsigned)__builtin_amdgcn_readfirstlane((unsigned)v); }
DEVI void granule(char* ws, unsigned off, unsigned v, u64 tag) { __hip_atomic_store((GAS u64*)(ws + off), (tag << 32) | v, RLX_AGENT); }
DEVI void granule(char* ws, unsigned off, float v, u64 tag) { granule(ws, off, __builtin_bit_cast(unsigned, v), tag); }

constexpr int XB = 32768, WB = 24576;
constexpr int WBASE = 3 * XB, MISC = 3 * XB + 2 * WB;
constexpr int STM = MISC, STR = MISC + 1024;
constexpr int CSL = MISC + 2048;
constexpr int WOB = CSL + 768;
constexpr int ZBUF = WOB, DUMMY = WOB;
constexpr int KVI = 143360;
constexpr int CNT = WOB + 13312;
constexpr int C_EPI = CNT, C_CS = CNT + 4, C_TAG = CNT + 8, C_DONE1 = CNT + 16, C_KVB = CNT + 20, C_KVD = CNT + 24, C_H = CNT + 28,
              C_FULL = CNT + 32, C_FREE = CNT + 48, C_QK = CNT + 80, C_MB = CNT + 84, C_M = CNT + 88, C_WO = CNT + 92, C_ZK = CNT + 96, C_H1 = CNT + 100;
constexpr int LDS_BYTES = 163840;
static_assert(CNT + 112 <= LDS_BYTES, "LDS");
DEVI unsigned lds_ld(const char* p) { return __hip_atomic_load((const LAS unsigned*)p, __ATOMIC_RELAXED, __HIP_MEMORY_SCOPE_WORKGROUP); }
DEVI void lds_st(char* p, unsigned v) { asm volatile("ds_write_b32 %0, %1" :: "v"((unsigned)(size_t)(LAS char*)p), "v"(v) : "memory"); }
DEVI unsigned lds_ld_now(const char* p) {
  unsigned v;
  asm volatile("ds_read_b32 %0, %1\n\ts_waitcnt lgkmcnt(0)" : "=v"(v) : "v"((unsigned)(size_t)(const LAS char*)p) : "memory");
  return v;
}
DEVI void lds_inc(char* p) {
  asm volatile("" ::: "memory");
  if (lane_id() == 0) __hip_atomic_fetch_add((LAS unsigned*)p, 1u, __ATOMIC_RELAXED, __HIP_MEMORY_SCOPE_WORKGROUP);
  asm volatile("" ::: "memory");
}
DEVI void lds_wait(const char* p, unsigned n) {
#pragma unroll 1
  while ((unsigned)__builtin_amdgcn_readfirstlane(lds_ld_now(p)) < n) __builtin_amdgcn_s_sleep(1);
  asm volatile("" ::: "memory");
}
DEVI u32x4 pair_mid(u32x2 a, u32x2 b) {
  auto s0 = __builtin_amdgcn_permlane32_swap(a[0], b[0], false, false);
  auto s1 = __builtin_amdgcn_permlane32_swap(a[1], b[1], false, false);
  return u32x4{s0[0], s1[0], s0[1], s1[1]};
}

DEVI int cs_col(bool gu, int lw, int lane) {
  const int g = lane < 48 ? lane >> 4 : 2;
  return (gu ? lw * 3 + g : (lw + 4 * g < 10 ? lw + 4 * g : lw)) * 16 + (lane & 15);
}

DEVI void publish_stats(char* smem, char* ws, int lane, u64 tag) {
  const unsigned v = lds_ld_now(smem + CSL + (lane & 15) * 4);
  if (lane < 16) granule(ws, ST_OFF + blockIdx.x * 128 + lane * 8, v, tag);
}

typedef __attribute__((address_space(4))) const u64 ka_t;

template <int ROLE>
DEVI void g1_compute(char* smem, rsrc_t rws, char* ws, int rt, int ct, int wid, int lane, u64 tag) {
  constexpr int NT = ROLE == 0 ? 6 : 5;
  constexpr int ROWB = ROLE == 0 ? 192 : 320;
  const int tg = wid & 3, ch = wid >> 2;
  const int fr = lane & 15, g = lane >> 4;
  unsigned xa[2];
#pragma unroll
  for (int kk = 0; kk < 2; ++kk) xa[kk] = (tg * 64 + fr) * 128 + (((kk * 4 + g) ^ (fr >> 1)) << 4);
  unsigned wa[NT];
  {
    const int q = (lane & 15) >> 2, p = lane & 3;
#pragma unroll
    for (int j = 0; j < NT; ++j) {
      int blk = 0, c;
      if (ROLE == 0) { blk = (j & 1) * 12288; c = (j < 4 ? ch * 4 + (j >> 1) : 2 + ch) * 32 + p * 8; }
      else if (ROLE == 1) c = ch * 160 + p * 40 + j * 8;
      else {
        if (j < 4) c = (j >> 1) * 160 + (ch * 2 + (j & 1)) * 40 + p * 8;
        else c = (p >> 1) * 160 + (ch * 2 + (p & 1)) * 40 + 32;
      }
      c ^= (g & 1) << 5;
      wa[j] = WBASE + blk + (g * 8 + q) * ROWB + c;
    }
  }
  f32x4 acc[4][NT];
#pragma unroll
  for (int i = 0; i < 4; ++i)
#pragma unroll
    for (int j = 0; j < NT; ++j) acc[i][j] = f32x4{0.f, 0.f, 0.f, 0.f};
  bf16x8 xf[2][4], wf[NT];
  auto rdw = [&](int j, int b, int kk) {
    const char* p = smem + b * WB + wa[j] + kk * 32 * ROWB;
    wf[j] = cat8(tr_read(p), tr_read(p + 4 * ROWB));
  };
  auto rdx = [&](int set, unsigned xaddr, int i) { xf[set][i] = *(const bf16x8*)(smem + xaddr + i * 2048); };
  auto mmj = [&](int set, int j) {
#pragma unroll
    for (int ii = 0; ii < 4; ++ii) {
      const int i = (j & 1) ? 3 - ii : ii;
      acc[i][j] = ROLE == 2 ? mfma16(xf[set][i], wf[j], acc[i][j]) : mfma16(wf[j], xf[set][i], acc[i][j]);
    }
  };
#pragma unroll
  for (int j = 0; j < NT; ++j) {
    asm volatile("" : "+v"(wa[j]));
#pragma unroll
    for (int i = 0; i < 4; ++i) asm volatile("" : "+v"(acc[i][j]));
  }
  asm volatile("" : "+v"(xa[0]), "+v"(xa[1]));
  bar();
#pragma unroll
  for (int i = 0; i < 4; ++i) rdx(0, xa[0], i);
#pragma unroll
  for (int j = 0; j < NT; ++j) rdw(j, 0, 0);
  __builtin_amdgcn_sched_barrier(0);
  unsigned xcur = 0;
  auto half = [&](int b, int skip) {
    const unsigned xnext = xcur == 2 * XB ? 0 : xcur + XB;
    const unsigned a1 = xa[1] + xcur, a0 = xa[0] + xnext;
    auto SB = [&]() { __builtin_amdgcn_sched_barrier(0); };
#pragma unroll
    for (int j = 0; j < NT; ++j) {
      mmj(0, j);
      SB();
      rdw(j, b, 1);
      if (j < 4) rdx(1, a1, j);
      SB();
    }
    mmj(1, 0);
    SB();
    if (b == 0) asm volatile("s_waitcnt lgkmcnt(0)\n\ts_barrier" ::: "memory");
    else asm volatile("s_cmp_lg_u32 %0, 0\n\ts_cbranch_scc1 1f\n\ts_waitcnt lgkmcnt(0)\n\ts_barrier\n1:" :: "s"(skip) : "scc", "memory");
    SB();
#pragma unroll
    for (int j = 0; j < NT; ++j) {
      if (j > 0) mmj(1, j);
      SB();
      rdw(j, b ^ 1, 0);
      if (j < 4) rdx(0, a0, j);
      SB();
    }
    xcur = xnext;
  };
#pragma unroll 1
  for (int t = 0; t < 10; t += 2) {
    half(0, 0);
    half(1, t & 8);
  }
  lds_inc(smem + C_DONE1);
  lane = lane_id();
  lds_wait(smem + C_CS, 4);

  if (ROLE == 0) {
    if (ch == 1) __builtin_amdgcn_s_setprio(2); else __builtin_amdgcn_s_setprio(1);
    const unsigned so = AF_OFF + ((rt * 4 + tg) * NS2 + ct * 3) * 4096;
    f32x4 cs[6];
#pragma unroll
    for (int j = 0; j < 6; ++j) cs[j] = *(const f32x4*)(smem + CSL + (j & 1) * 384 + (j < 4 ? ch * 4 + (j >> 1) : 2 + ch) * 64 + (lane >> 4) * 16);
    float nm[4], rs[4], sd[4];
#pragma unroll
    for (int i = 0; i < 4; ++i) {
      nm[i] = -*(const float*)(smem + STM + (tg * 64 + i * 16 + (lane & 15)) * 4);
      rs[i] = *(const float*)(smem + STR + (tg * 64 + i * 16 + (lane & 15)) * 4);
      sd[i] = __builtin_amdgcn_rcpf(rs[i]);
    }
    auto hq = [&](int i, int u) {
      const f32x2 nm2 = {nm[i], nm[i]}, rs2 = {rs[i], rs[i]}, sd2 = {sd[i], sd[i]};
      u32x2 o;
#pragma unroll
      for (int r = 0; r < 2; ++r) {
        const f32x2 gv = rs2 * __builtin_elementwise_fma(f32x2{cs[2 * u][2 * r], cs[2 * u][2 * r + 1]}, nm2, f32x2{acc[i][2 * u][2 * r], acc[i][2 * u][2 * r + 1]});
        const f32x2 uv = __builtin_elementwise_fma(f32x2{cs[2 * u + 1][2 * r], cs[2 * u + 1][2 * r + 1]}, nm2, f32x2{acc[i][2 * u + 1][2 * r], acc[i][2 * u + 1][2 * r + 1]});
        const float c1 = -2.f * 0.7978845608f * 1.4426950409f, c2 = c1 * 0.044715f;
        const f32x2 p = gv * __builtin_elementwise_fma(gv * gv, f32x2{c2, c2}, f32x2{c1, c1});
        const f32x2 d = __builtin_elementwise_fma(f32x2{__builtin_amdgcn_exp2f(p[0]), __builtin_amdgcn_exp2f(p[1])}, sd2, sd2);
        const f32x2 h = (gv * uv) * f32x2{__builtin_amdgcn_rcpf(d[0]), __builtin_amdgcn_rcpf(d[1])};
        o[r] = pack2(h[0], h[1]);
      }
      return o;
    };
#pragma unroll
    for (int i = 0; i < 4; ++i) {
      const u32x2 a = hq(i, 0), b = hq(i, 1);
      st16(rws, lane * 16 + i * 1024, so + ch * 8192, u32x4{a[0], a[1], b[0], b[1]});
    }
    __builtin_amdgcn_sched_barrier(0);
    __builtin_amdgcn_s_setprio(0);
    u32x4 md[2];
#pragma unroll
    for (int i = 0; i < 2; ++i) { md[i] = pair_mid(hq(2 * i, 2), hq(2 * i + 1, 2)); asm volatile("" : "+v"(md[i])); }
#pragma unroll
    for (int i = 0; i < 2; ++i) st16(rws, (lane & 31) * 16 + (lane >> 5) * 1024 + i * 2048, so + 4096 + ch * 512, md[i]);
    __builtin_amdgcn_sched_barrier(0);
    asm volatile("s_waitcnt vmcnt(2)" ::: "memory");
    if (lane < 8) post(ws, (rt * 4 + tg) * 8 + lane, SL_H + ch * 18 + ct, tag);
  } else if (ROLE == 1) {
    f32x4 cs[5];
#pragma unroll
    for (int j = 0; j < 5; ++j) cs[j] = *(const f32x4*)(smem + CSL + (ch * 80 + (lane >> 4) * 20 + j * 4) * 4);
#pragma unroll
    for (int i = 0; i < 4; ++i) {
      const float nm = -*(const float*)(smem + STM + (tg * 64 + i * 16 + (lane & 15)) * 4);
      const float rs = *(const float*)(smem + STR + (tg * 64 + i * 16 + (lane & 15)) * 4);
#pragma unroll
      for (int j = 0; j < 5; ++j) {
        acc[i][j] = phi4(__builtin_elementwise_fma(__builtin_elementwise_fma(cs[j], splat4(nm), acc[i][j]), splat4(rs), splat4(1e-6f)));
        asm volatile("" : "+v"(acc[i][j]));
      }
    }
    unsigned so = AF_OFF + ((rt * 4 + tg) * NS2 + 54 + ct * 5) * 4096 + ch * 12288 + lane * 16;
    unsigned sm = AF_OFF + ((rt * 4 + tg) * NS2 + 56 + ct * 5) * 4096 + ch * 512 + (lane & 31) * 16 + (lane >> 5) * 1024;
    unsigned za = ZBUF + (ch * 4 + (lane >> 4)) * 80;
    asm volatile("" : "+v"(so), "+v"(sm), "+v"(za));
    __builtin_amdgcn_s_setprio(0);
    {
      const unsigned zo = (lane >> 4) * 256 + ((lane & 15) < 10 ? lane & 15 : 0) * 16;
      u32x4 zp[8];
      unsigned todo = 0xff;
#pragma unroll 1
      for (int spin = 0; spin < (1 << 18) && todo; ++spin) {
        const unsigned ask = todo == 0xff && (spin & 3) != 3 ? 1u << rt : todo;
#pragma unroll
        for (int k = 0; k < 8; ++k)
          if (ask >> k & 1) zp[k] = ld16(rws, zo, ZP_OFF + (ct * 8 + wid) * 8192 + k * 1024);
        drain();
#pragma unroll
        for (int k = 0; k < 8; ++k)
          if ((ask >> k & 1) && __all(zp[k][1] == (unsigned)tag && zp[k][3] == (unsigned)tag)) todo &= ~(1u << k);
      }
      float za0 = 0.f, za1 = 0.f;
#pragma unroll
      for (int k = 0; k < 8; ++k) { za0 += as_f(zp[k][0]); za1 += as_f(zp[k][2]); }
      za0 += __shfl_xor(za0, 16); za1 += __shfl_xor(za1, 16);
      za0 += __shfl_xor(za0, 32); za1 += __shfl_xor(za1, 32);
      if (lane < 10) *(f32x2*)(smem + ZBUF + wid * 80 + lane * 8) = f32x2{za0, za1};
      lds_inc(smem + C_ZK);
    }
    lds_wait(smem + C_ZK, 8);
    f32x4 z[5];
#pragma unroll
    for (int j = 0; j < 5; ++j) z[j] = *(const f32x4*)(smem + za + j * 16);
    u32x2 mid[4];
#pragma unroll
    for (int i = 0; i < 4; ++i) {
      f32x4 d4 = acc[i][0] * z[0];
#pragma unroll
      for (int j = 1; j < 5; ++j) d4 = __builtin_elementwise_fma(acc[i][j], z[j], d4);
      const float inv = __builtin_amdgcn_rcpf(((d4[0] + d4[1]) + (d4[2] + d4[3])) + 1e-6f);
      u32x2 a[5];
#pragma unroll
      for (int j = 0; j < 5; ++j) a[j] = pack4(acc[i][j] * inv);
      st16(rws, so, i * 1024, u32x4{a[0][0], a[0][1], a[1][0], a[1][1]});
      st16(rws, so, i * 1024 + 4096, u32x4{a[2][0], a[2][1], a[3][0], a[3][1]});
      mid[i] = a[4];
      if (i & 1) st16(rws, sm, (i - 1) * 1024, pair_mid(mid[i - 1], mid[i]));
    }
    lds_inc(smem + C_EPI);
    lds_wait(smem + C_EPI, 8);
  } else {
    bf16x8 op[2][5];
    __builtin_amdgcn_s_setprio(2);
    const unsigned isv = bitmask(lane, 3);
    float ncs[5];
#pragma unroll
    for (int j = 0; j < 5; ++j) {
      const int f = lane & 15;
      const int col = j < 4 ? (j >> 1) * 80 + (ch * 2 + (j & 1)) * 20 + f : (f >> 3) * 80 + (ch * 2 + ((f >> 2) & 1)) * 20 + 16 + (f & 3);
      ncs[j] = -*(const float*)(smem + CSL + col * 4);
    }
    const float eps4 = __builtin_bit_cast(float, sel(isv, 0u, __builtin_bit_cast(unsigned, 1e-6f)));
    f32x4 mn[4], rsv[4];
#pragma unroll
    for (int i = 0; i < 4; ++i) {
      mn[i] = *(const f32x4*)(smem + STM + (tg * 64 + i * 16 + (lane >> 4) * 4) * 4);
      rsv[i] = *(const f32x4*)(smem + STR + (tg * 64 + i * 16 + (lane >> 4) * 4) * 4);
    }
    auto mkop = [&](int hh, int j) {
      const f32x4 ep = splat4(j < 2 ? 1e-6f : j < 4 ? 0.f : eps4);
      const f32x4 a = __builtin_elementwise_fma(__builtin_elementwise_fma(mn[2 * hh], splat4(ncs[j]), acc[2 * hh][j]), rsv[2 * hh], ep);
      const f32x4 b = __builtin_elementwise_fma(__builtin_elementwise_fma(mn[2 * hh + 1], splat4(ncs[j]), acc[2 * hh + 1][j]), rsv[2 * hh + 1], ep);
      u32x2 pa, pb;
      if (j == 2 || j == 3) { pa = pack4(a); pb = pack4(b); }
      else {
        pa = pack4(phi4(a)); pb = pack4(phi4(b));
        if (j == 4) {
          const u32x2 ra = pack4(a), rb = pack4(b);
          pa = u32x2{sel(isv, ra[0], pa[0]), sel(isv, ra[1], pa[1])}; pb = u32x2{sel(isv, rb[0], pb[0]), sel(isv, rb[1], pb[1])};
        }
      }
      op[hh][j] = __builtin_bit_cast(bf16x8, u32x4{pa[0], pa[1], pb[0], pb[1]});
    };
    const int fr2 = lane & 15, g2 = lane >> 4, q2 = fr2 >> 2, p2 = lane & 3, tile = wid & 3;
    f32x4 y[8];
#pragma unroll
    for (int n = 0; n < 8; ++n) y[n] = f32x4{0.f, 0.f, 0.f, 0.f};
    const bf16x8 ones = __builtin_bit_cast(bf16x8, u32x4{0x3f803f80u, 0x3f803f80u, 0x3f803f80u, 0x3f803f80u});
#pragma unroll
    for (int hh = 0; hh < 2; ++hh) {
      mkop(hh, 0); mkop(hh, 1); mkop(hh, 4);
      const u32x4 t4 = __builtin_bit_cast(u32x4, op[hh][4]);
      u32x4 x4, r4;
#pragma unroll
      for (int i = 0; i < 4; ++i) {
        x4[i] = sel(isv, t4[i], 0x3f803f80u);
        r4[i] = (unsigned)__builtin_amdgcn_update_dpp(0, (int)t4[i], 0x128, 0xf, 0xf, true);
      }
      const bf16x8 X4 = __builtin_bit_cast(bf16x8, x4), R4 = __builtin_bit_cast(bf16x8, r4);
      y[2] = mfma16(X4, op[hh][0], y[2]);
      y[3] = mfma16(X4, op[hh][1], y[3]);
      y[7] = mfma16(ones, op[hh][4], y[7]);
      y[6] = mfma16(X4, R4, y[6]);
    }
    const f32x4 t2 = sel4(bitmask(lane, 4), y[3], y[2]);
    if (lane < 40)
      granule(ws, ZP_OFF + (ct * 4 + ch * 2 + (g2 < 2 ? g2 : q2)) * 8192 + (rt * 4 + tg) * 256 + (g2 < 2 ? fr2 : 16 + p2) * 8, g2 < 2 ? t2[0] : y[7][0], tag);
    __builtin_amdgcn_s_setprio(0);
    __builtin_amdgcn_sched_barrier(0);
#pragma unroll
    for (int hh = 0; hh < 2; ++hh) {
      mkop(hh, 2); mkop(hh, 3);
      y[0] = mfma16(op[hh][2], op[hh][0], y[0]);
      y[1] = mfma16(op[hh][3], op[hh][1], y[1]);
      y[4] = mfma16(op[hh][2], op[hh][4], y[4]);
      y[5] = mfma16(op[hh][3], op[hh][4], y[5]);
    }
    f32x4* red = (f32x4*)(smem + 40960);
    f32x4* mine = red + (tg * 8 + ch * 4) * 64 + lane;
    mine[0] = y[0];
    mine[64] = y[1];
    mine[128] = t2;
    mine[192] = sel4(isv, y[6], sel4(bitmask(lane, 2), y[5], y[4]));
    lds_inc(smem + C_KVB);
    lds_wait(smem + C_KVB, 8);
    const f32x4* src = red + wid * 64 + lane;
    const f32x4 s = (src[0] + src[512]) + (src[1024] + src[1536]);
    const u32x2 sp = pack4(s);
    granule(ws, KVP_OFF + (rt * 8 + ct) * 8192 + (wid * 64 + lane) * 8, sp[0], tag);
    granule(ws, KVP_OFF + (rt * 8 + ct) * 8192 + 4096 + (wid * 64 + lane) * 8, sp[1], tag);
    *(u32x2*)(smem + KVI + (wid * 64 + lane) * 8) = u32x2{0u, 0u};
    lds_inc(smem + C_KVD);
    lds_wait(smem + C_WO, 4);
    bf16x8 bw[3];
#pragma unroll
    for (int m = 0; m < 3; ++m) {
      const char* pw = smem + WOB + (g2 * 20 + m * 8 + q2) * 160 + (wid < 5 ? wid : 0) * 32 + p2 * 8;
      bw[m] = cat8(tr_read(pw), tr_read(pw + (m < 2 ? 4 * 160 : 0)));
    }
    int h = 2 * ch, d, e = 4 * g2;
    bool ok = true;
    if (tile < 2) { h += tile; d = fr2; }
    else if (tile == 2) { h += g2 & 1; d = fr2; e = 16; ok = g2 >= 2; }
    else { h += q2 & 1; d = 16 + p2; if (fr2 >= 8) { e = 16; ok = g2 == 2 + (q2 & 1); } }
    int dst = ok ? (h * 20 + d) * 48 + e * 2 : 81 * 48;
    int aoff = q2 == g2 ? (g2 * 20 + p2) * 48 : 80 * 48, astep = q2 == g2 ? 4 * 48 : 0;
    asm volatile("" : "+v"(dst), "+v"(aoff), "+v"(astep));
    u32x2 pl[8], ph[8];
    unsigned todo = 0xff;
#pragma unroll 1
    for (int spin = 0; spin < (1 << 18) && todo; ++spin) {
#pragma unroll
      for (int k = 0; k < 8; ++k)
        if (todo >> k & 1) {
          pl[k] = __builtin_amdgcn_raw_buffer_load_b64(rws, (wid * 64 + lane) * 8, KVP_OFF + (k * 8 + ct) * 8192, 16);
          ph[k] = __builtin_amdgcn_raw_buffer_load_b64(rws, (wid * 64 + lane) * 8 + 4096, KVP_OFF + (k * 8 + ct) * 8192, 16);
        }
      drain();
#pragma unroll
      for (int k = 0; k < 8; ++k)
        if (__all(pl[k][1] == (unsigned)tag && ph[k][1] == (unsigned)tag)) todo &= ~(1u << k);
    }
    f32x4 t = {0.f, 0.f, 0.f, 0.f};
#pragma unroll
    for (int k = 0; k < 8; ++k) t += f32x4{bf_lo(pl[k][0]), bf_hi(pl[k][0]), bf_lo(ph[k][0]), bf_hi(ph[k][0])};
    lds_wait(smem + C_KVD, 8);
    *(u32x2*)(smem + KVI + dst) = pack4(t);
    lds_inc(smem + C_MB);
    if (wid < 5) {
      lds_wait(smem + C_MB, 8);
      u32x2 a[5];
#pragma unroll
      for (int j = 0; j < 5; ++j) {
        f32x4 c = {0.f, 0.f, 0.f, 0.f};
#pragma unroll
        for (int m = 0; m < 3; ++m) c = mfma16(*(const bf16x8*)(smem + KVI + aoff + j * astep + m * 16), bw[m], c);
        a[j] = pack4(c);
      }
      const unsigned to = WF_OFF + (rt * NS2 + 54 + (ct >> 1) * 5) * 5120 + wid * 1024;
      st16(rws, lane * 16, to + (ct & 1) * 15360, u32x4{a[0][0], a[0][1], a[1][0], a[1][1]});
      st16(rws, lane * 16, to + (ct & 1) * 15360 + 5120, u32x4{a[2][0], a[2][1], a[3][0], a[3][1]});
      const u32x4 md = pair_mid(a[4], a[4]);
      if (lane < 32) st16(rws, lane * 16, to + 10240 + (ct & 1) * 512, md);
    }
  }
}

DEVI void spare_relayout(char* smem, rsrc_t rws, const char* __restrict__ wd, char* ws, int id, int wid, int lane) {
  const rsrc_t rs = mk_rsrc(wd, F_ * 1280);
  const int it0 = id * 27 / 2, n = (id + 1) * 27 / 2 - it0;
#pragma unroll 1
  for (int k = 11 - wid; k < n; k += 12) {
    const int s = (it0 + k) >> 2, c = (it0 + k) & 3;
    const bool mid = s % 3 == 1;
#pragma unroll
    for (int i = 0; i < 10; ++i) {
      const int ci = i * 64 + lane, r = ci / 20, g = r >> 3, h = (r >> 2) & 1;
      const int row = (mid ? 16 * (g >> 1) + 4 * (g & 1) + 8 * h : 4 * g + 16 * h) + (r & 3);
      dma16<0>(rs, smem + k * 10240 + i * 1024, row * 1280 + (ci % 20) * 16, s * 32 * 1280 + c * 320);
    }
  }
  bar();
  const u64 tag = uni64(*(const u64*)(smem + C_TAG));
  if (wid == 8) publish_stats(smem, ws, lane, tag);
  drain();
#pragma unroll 1
  for (int k = 11 - wid; k < n; k += 12) {
    const int s = (it0 + k) >> 2, c = (it0 + k) & 3;
    const char* pp = smem + k * 10240 + ((lane >> 4) * 8 + ((lane & 15) >> 2)) * 320 + (lane & 3) * 8;
    bf16x8 v[10];
#pragma unroll
    for (int j = 0; j < 10; ++j) v[j] = cat8(tr_read(pp + j * 32), tr_read(pp + j * 32 + 4 * 320));
#pragma unroll
    for (int j = 0; j < 10; ++j)
      st16(rws, lane * 16 + (j % 5) * 1024, WF_OFF + ((c * 2 + j / 5) * NS2 + s) * 5120, __builtin_bit_cast(u32x4, v[j]));
  }
  if (arrive_last(smem + C_EPI, 12)) {
#pragma unroll
    for (int k = 0; k < 4; ++k) post(ws, k * 64 + lane, SL_RL + id, tag);
    drain();
    lds_st(smem + C_QK, 1);
  }
}

constexpr unsigned CODE_BYTES = 45000;
DEVI int g2_step(int gI, int kq) {
  return gI < 9 ? 6 * gI + 3 * (kq >> 1) + 2 * (kq & 1) : gI < 13 ? 3 * (4 * (gI - 9) + kq) + 1 : gI == 13 ? 3 * (16 + (kq & 1)) + 1 : 54 + 5 * kq + (gI - 14);
}
DEVI void loader(char* smem, u64 k0, u64 k1, u64 k2, u64 k3, u64 k4, u64 k5, u64 k7, int rt, int ct, int sid, int r2, int c2, int lw, int lane) {
  const bool gu = ct < 18, g1 = ct < 30, iskv = ct >= 22 && g1;
  const int wid = lw + 8; (void)wid;
  const unsigned wstr = gu ? F_ * 2 : 1920 * 2;
  unsigned wo_[6];
  const unsigned cpr = gu ? 12 : 20, mag = gu ? 5462 : 3277;
  const unsigned cmul = gu ? 192 : ct < 22 ? 320 : 160, cadd = gu ? 0 : ct < 22 ? -5760 : -2240, jump = ct < 22 ? 0 : 1120;
  const unsigned cbase = ct * cmul + cadd + (gu ? (lw & 1) * 32 : lw * 16) * wstr;
#pragma unroll
  for (int i = 0; i < 5; ++i) {
    const unsigned ci = i * 64 + lane, r = __umul24(ci, mag) >> 16;
    const unsigned lc = (ci - __umul24(r, cpr)) ^ ((r >> 2) & 2);
    wo_[i] = __umul24(r, wstr) + cbase + lc * 16 + __umul24((lc + 6) >> 4, jump);
  }
  wo_[5] = wo_[2] + 16 * wstr;
  unsigned xo[2];
#pragma unroll
  for (int i = 0; i < 2; ++i) xo[i] = __umul24(rt * 256 + lw * 64 + (lane >> 3), 1280) + (((lane & 7) ^ (i * 4 + (lane >> 4))) << 4);
  asm volatile("" : "+s"(k0), "+s"(k1), "+s"(k3), "+s"(k4), "+s"(k7) : "v"(wo_[0]), "v"(wo_[1]), "v"(wo_[2]), "v"(wo_[3]), "v"(wo_[4]), "v"(xo[0]), "v"(xo[1]));
  char* ws = (char*)(GAS char*)k7;
  const rsrc_t rws = mk_rsrc(ws, WS_END);
  const char* box = ws + BOX_OFF + (r2 * 8 + c2) * 1024;
  const unsigned wso = WF_OFF + c2 * NS2 * 5120;
  auto ldw = [&](int gI) {
#pragma unroll
    for (int j = 0; j < 5; ++j) dma16<16>(rws, smem + ((gI % 7) * 4 + lw) * 5120 + j * 1024, lane * 16, wso + g2_step(gI, lw) * 5120 + j * 1024);
  };
  u32x4 wr[7][5];
  auto ldr = [&](int gI) {
#pragma unroll
    for (int j = 0; j < 5; ++j) wr[gI - 7][j] = ld16(rws, lane * 16 + j * 1024, wso + g2_step(gI, lw) * 5120);
  };
  auto thr = [&]() { __builtin_amdgcn_sched_barrier(0); asm volatile("s_waitcnt vmcnt(%0)" :: "n"(5) : "memory"); __builtin_amdgcn_sched_barrier(0); };
  auto inc = [&](int w) { if (lane == 0) asm volatile("ds_add_u32 %0, %1" :: "v"((unsigned)(size_t)(LAS char*)(smem + w)), "v"(1u) : "memory"); };
  u64 tag;
  bool rlok = false;
  if (__builtin_expect(g1, 1)) {
    const rsrc_t rw = mk_rsrc((const void*)(gu ? (lw >= 2 ? k4 : k3) : k1), D_ * wstr);
    const rsrc_t rx = mk_rsrc((const void*)k0, S_ * D_ * 2);
    char* wdst = smem + WBASE + lw * (gu ? 6144 : 5120);
    auto stage_w = [&](int t, int b) {
#pragma unroll
      for (int n = 0; n < 6; ++n)
        if (n < 5 || gu) dma16<0>(rw, wdst + b * WB + n * 1024, wo_[n], t * 64 * wstr);
    };
    auto stage_x = [&](int t, unsigned xb) {
#pragma unroll
      for (int n = 0; n < 8; ++n) dma16<0>(rx, smem + xb + (lw * 8 + n) * 1024, xo[n & 1], n * 10240 + t * 128);
    };
    auto mtouch = [&](int s0, int s1, bool wo_too) {
      const int n = lw == 0 ? 8 * (s1 - s0) : lw == 3 ? (wo_too ? 27 : 1) : (36 * (s1 - s0) + 4) / 5, tot = lw == 0 ? 19200 : lw == 3 ? 6400 : 17280;
      int ln = (lw == 0 ? 1920 * s0 : lw == 3 ? 0 : 1728 * s0) + (lane < n ? lane : 0) * 240 + rt * 30 + ct;
      ln = ln < tot ? ln : tot - 1;
      __builtin_amdgcn_global_load_lds((const GAS void*)((lw == 0 ? k1 : lw == 1 ? k3 : lw == 2 ? k4 : k2) + (u64)ln * 128), (LAS void*)(smem + DUMMY), 4, 0, 0);
    };
    mtouch(1, 4, false);
    stage_w(0, 0);
    stage_x(0, 0);
    __builtin_amdgcn_s_sleep(2);
    stage_w(1, 1);
    stage_x(1, XB);
    if (gu) asm volatile("s_waitcnt vmcnt(14)" ::: "memory");
    else asm volatile("s_waitcnt vmcnt(13)" ::: "memory");
    bar();
    tag = uni64(*(const u64*)(smem + C_TAG));
    stage_x(2, 2 * XB);
    if (lw == 0) publish_stats(smem, ws, lane, tag);
    const int rowb = gu ? 192 : 320, col = cs_col(gu, lw, lane);
    unsigned ca[3];
#pragma unroll
    for (int k = 0; k < 3; ++k) {
      const int tile = gu ? (lw & 1) * 3 + k : (lw + 4 * k < 10 ? lw + 4 * k : lw);
      ca[k] = WBASE + (gu ? (lw >> 1) * 12288 : 0) + ((lane >> 4) * 8 + ((lane & 15) >> 2)) * rowb + ((tile * 32 + (lane & 3) * 8) ^ ((lane & 16) << 1));
    }
    const unsigned m4 = bitmask(lane, 4), m5 = bitmask(lane, 5);
    auto csum = [&](int b) {
      const bf16x8 ones = __builtin_bit_cast(bf16x8, u32x4{0x3f803f80u, 0x3f803f80u, 0x3f803f80u, 0x3f803f80u});
      bf16x8 w[3][2];
#pragma unroll
      for (int k = 0; k < 3; ++k) {
        const unsigned p0 = (unsigned)(size_t)(LAS char*)smem + b * WB + ca[k];
        s16x4 r0, r1, r2_, r3;
        asm volatile("ds_read_b64_tr_b16 %0, %4\n\tds_read_b64_tr_b16 %1, %5\n\tds_read_b64_tr_b16 %2, %6\n\tds_read_b64_tr_b16 %3, %7\n\ts_waitcnt lgkmcnt(0)"
                     : "=&v"(r0), "=&v"(r1), "=&v"(r2_), "=&v"(r3) : "v"(p0), "v"(p0 + 4 * rowb), "v"(p0 + 32 * rowb), "v"(p0 + 36 * rowb) : "memory");
        w[k][0] = cat8(r0, r1);
        w[k][1] = cat8(r2_, r3);
      }
      f32x4 c[3];
#pragma unroll
      for (int k = 0; k < 3; ++k) c[k] = mfma16(ones, w[k][0], f32x4{0.f, 0.f, 0.f, 0.f});
#pragma unroll
      for (int k = 0; k < 3; ++k) c[k] = mfma16(ones, w[k][1], c[k]);
      const float c0 = c[0][0], c1 = c[1][0], c2_ = c[2][0];
      float r = as_f(sel(m5, __builtin_bit_cast(unsigned, c2_), sel(m4, __builtin_bit_cast(unsigned, c1), __builtin_bit_cast(unsigned, c0))));
      asm volatile("" : "+v"(r));
      return r;
    };
    unsigned xb = 0;
#pragma unroll 1
    for (int t = -1; t < 7; ++t) {
      if (t >= 0) {
        if (t == 1) asm volatile("s_waitcnt vmcnt(9)" ::: "memory");
        else asm volatile("s_waitcnt vmcnt(8)" ::: "memory");
        bar();
        if (t == 6) __builtin_amdgcn_global_load_lds((const GAS void*)(box + SL_RL * 8 + (lane & 31) * 4), (LAS void*)(smem + DUMMY + 256 + lw * 256), 4, 0, 16);
        stage_w(t + 2, t & 1);
        stage_x(t + 3, xb);
        xb = xb == 2 * XB ? 0 : xb + XB;
      }
      if (t == 0) mtouch(4, 10, true);
      if (t + 1 == rt) granule(ws, CS_OFF + ((ct * 8 + rt) * 192 + col) * 8, csum(rt & 1), tag);
    }
    asm volatile("s_waitcnt vmcnt(13)" ::: "memory");
    rlok = __all(lds_ld_now(smem + DUMMY + 256 + lw * 256 + lane * 4) == ((lane & 1) ? (unsigned)(tag >> 32) : (unsigned)tag));
    u32x4 st = ld16(rws, lane * 16, ST_OFF + (rt * 256 + lw * 64) * 16);
    if (rlok) { ldr(7); __builtin_amdgcn_sched_barrier(0); asm volatile("s_waitcnt vmcnt(14)" ::: "memory"); }
    else asm volatile("s_waitcnt vmcnt(9)" ::: "memory");
    bar();
    stage_w(9, 1);
    if (rlok) { ldr(8); ldr(9); }
    __builtin_amdgcn_sched_barrier(0);
    float cst = csum(0);
    u32x2 cv[8];
#pragma unroll
    for (int k = 0; k < 8; ++k) cv[k] = __builtin_amdgcn_raw_buffer_load_b64(rws, col * 8, CS_OFF + (ct * 8 + k) * 1536, 16);
    __builtin_amdgcn_sched_barrier(0);
    if (rlok) asm volatile("s_waitcnt vmcnt(18)" ::: "memory");
    else asm volatile("s_waitcnt vmcnt(8)" ::: "memory");
    bar();
    cst += csum(1);
#pragma unroll 1
    for (int spin = 0; spin < (1 << 18); ++spin) {
      bool ok = st[1] == (unsigned)tag && st[3] == (unsigned)tag;
#pragma unroll
      for (int k = 0; k < 8; ++k) ok &= cv[k][1] == (unsigned)tag;
      if (__all(ok)) break;
      asm volatile("" ::: "memory");
      st = ld16(rws, lane * 16, ST_OFF + (rt * 256 + lw * 64) * 16);
#pragma unroll
      for (int k = 0; k < 8; ++k) cv[k] = __builtin_amdgcn_raw_buffer_load_b64(rws, col * 8, CS_OFF + (ct * 8 + k) * 1536, 16);
    }
#pragma unroll
    for (int k = 0; k < 8; ++k) cst += as_f(cv[k][0]);
    lds_st(smem + STM + (lw * 64 + lane) * 4, st[0]);
    lds_st(smem + STR + (lw * 64 + lane) * 4, st[2]);
    lds_st(smem + CSL + col * 4, __builtin_bit_cast(unsigned, cst));
    inc(C_CS);
    if (iskv) {
      const rsrc_t ro = mk_rsrc((const void*)k2, D_ * 1280);
#pragma unroll 1
      for (int i = lw; i < 13; i += 4) {
        const int ci = i * 64 + lane, cc = ci < 800 ? ci : 799;
        dma16<0>(ro, smem + WOB + i * 1024, (cc / 10) * 1280 + (cc % 10) * 16, (ct - 22) * 80 * 1280 + rt * 160);
      }
    }
  } else {
    spare_relayout(smem, rws, (const char*)k5, ws, sid, wid, lane);
    tag = uni64(*(const u64*)(smem + C_TAG));
    if (sid == 15 && lw == 0) {
#pragma unroll 1
      for (int spin = 0; spin < (1 << 18); ++spin) {
        bool ok = true;
#pragma unroll
        for (int k = 0; k < 4; ++k) { const u32x4 v = ld16(rws, (k * 64 + lane) * 128, ST_OFF); ok &= v[1] == (unsigned)tag && v[3] == (unsigned)tag; }
        if (__all(ok)) break;
        __builtin_amdgcn_s_sleep(8);
        asm volatile("" ::: "memory");
      }
      if (lane == 0) __hip_atomic_store((GAS u64*)(ws + GEN_OFF), tag, RLX_AGENT);
    }
  }
  __builtin_amdgcn_s_setprio(0);
  if (!rlok) { await<4>(box + SL_RL * 8, 16, tag, lane); ldr(7); ldr(8); thr(); ldr(9); }
  ldw(4); thr(); ldw(5); thr();
  if (iskv) inc(C_WO);
  else { ldw(2); thr(); ldw(3); thr(); }
  ldr(10); thr(); ldr(11); thr();
  if (g1) { lds_wait(smem + C_DONE1, 8); lds_wait(smem + C_CS, 4); }
  ldw(0); thr(); ldw(1); thr(); ldw(6); thr();
  if (iskv) { lds_wait(smem + C_KVD, 8); ldw(2); thr(); ldw(3); thr(); }
  ldr(12);
  __builtin_amdgcn_sched_barrier(0);
  asm volatile("s_waitcnt vmcnt(5)" ::: "memory");
  lds_st(smem + C_FULL + lw * 4, 7);
  ldr(13);
  __builtin_amdgcn_sched_barrier(0);
  lane = lane_id();
  if (lw == 1) {
    if (!gu) {
      await<6>(box + SL_H * 8, 36, tag, lane);
      lds_st(smem + C_H, 1);
    }
    lds_wait(smem + C_H, 1);
    await<1>(box + SL_H1 * 8, 36, tag, lane);
    lds_st(smem + C_H1, 1);
  }
  const char* mbox = box + (SL_M + lw * 2) * 8;
  char* wdst = smem + lw * 5120 + lane * 16;
  auto wait_free = [&](unsigned n) {
#pragma unroll 1
    while (!__all(lds_ld_now(smem + C_FREE + lw * 8 + (lane & 1) * 4) >= n)) __builtin_amdgcn_s_sleep(1);
    asm volatile("" ::: "memory");
  };
  u64 mflag = 0;
#pragma unroll
  for (int gI = 7; gI < 14; ++gI) {
    wait_free(gI - 6);
#pragma unroll
    for (int j = 0; j < 5; ++j) *(u32x4*)(wdst + (gI % 7) * 20480 + j * 1024) = wr[gI - 7][j];
    asm volatile("" ::: "memory");
    lds_st(smem + C_FULL + lw * 4, gI + 1);
    if (gI == 10) mflag = __hip_atomic_load((const GAS u64*)mbox + (lane & 1), RLX_AGENT);
    __builtin_amdgcn_sched_barrier(0);
  }
  if (!__all(mflag == tag)) await<2>(mbox, 2, tag, lane);
  __builtin_amdgcn_fence(__ATOMIC_ACQUIRE, "wavefront");
#pragma unroll
  for (int gI = 14; gI < 19; ++gI) {
    wait_free(gI - 6);
    ldw(gI);
    if (gI >= 16) { asm volatile("s_waitcnt vmcnt(10)" ::: "memory"); lds_st(smem + C_FULL + lw * 4, gI - 1); }
  }
  asm volatile("s_waitcnt vmcnt(5)" ::: "memory");
  lds_st(smem + C_FULL + lw * 4, 18);
  drain();
  lds_st(smem + C_FULL + lw * 4, 19);
}

__global__ __launch_bounds__(768, 3) void fused(const char* a_x, const char* a_wqkv, const char* a_wo, const char* a_wg, const char* a_wu,
                                                const char* a_wd, char* a_out, char* a_ws) {
  extern __shared__ __attribute__((aligned(16))) char smem[];
  int lane = threadIdx.x & 63;
  const int wid = __builtin_amdgcn_readfirstlane(threadIdx.x >> 6);
  if (wid == 8) {
    u64 pc;
    asm volatile("s_getpc_b64 %0" : "=s"(pc));
    const unsigned off = ((blockIdx.x >> 3) * 16 + (lane & 15)) * 128;
    __builtin_amdgcn_global_load_lds((const GAS void*)((pc & ~127ull) + (off < CODE_BYTES ? off : 0u)), (LAS void*)(smem + DUMMY), 4, 0, 0);
  }
  ka_t* ka = (ka_t*)__builtin_amdgcn_kernarg_segment_ptr();
  asm volatile("" : "+s"(ka) :: "memory");
  u64 k0 = ka[0], k1 = ka[1], k2 = ka[2], k3 = ka[3], k4 = ka[4], k5 = ka[5], k6 = ka[6], k7 = ka[7];
  u64 gen;
  u32x4 v0, v1;
  if (wid < 8) {
    const GAS char* xr0 = (const GAS char*)k0 + (blockIdx.x * 8 + wid) * 1280;
    v0 = *(const GAS u32x4*)(xr0 + lane * 16);
    v1 = *(const GAS u32x4*)(xr0 + 1024 + (lane & 15) * 16);
    gen = __hip_atomic_load((const GAS u64*)(k7 + GEN_OFF), RLX_AGENT);
  }
  int bid = blockIdx.x;
  asm volatile("" : "+s"(bid) :: "memory");
  const int xcd = bid & 7, idx = bid >> 3;
  const int xk = xcd & 3;
  const int tn = xcd < 4 ? xk * 32 + idx : idx < 4 ? 128 + xk * 4 + idx : idx < 12 ? 140 + xk * 8 + idx : idx < 28 ? 164 + xk * 16 + idx : 240;
  const int rt = tn & 7, ct = tn >> 3;
  const int sid = xk * 4 + idx - 28;
  const int r2 = (xcd >> 1) * 8 + (idx >> 2), c2 = (xcd & 1) * 4 + (idx & 3);
  if (wid >= 8) {
    __builtin_amdgcn_s_setprio(3);
    loader(smem, k0, k1, k2, k3, k4, k5, k7, rt, ct, sid, r2, c2, wid - 8, lane);
  }
  asm volatile("" : "+s"(k0), "+s"(k7));
  const char* x = (const char*)(const GAS char*)k0;
  const char* wd = (const char*)(const GAS char*)k5;
  char* out = (char*)(GAS char*)k6;
  char* ws = (char*)(GAS char*)k7;
  const rsrc_t rws = mk_rsrc(ws, WS_END);
  const char* box = ws + BOX_OFF + (r2 * 8 + c2) * 1024;

  unsigned oo, ra;
  u32x4 xr;
  auto fin_prep = [&]() {
    const int tid = (wid < 10 ? wid : 0) * 64 + lane_id();
    const int tok = tid / 10, m8 = tid % 10;
    oo = (r2 * 64 + tok) * 1280 + c2 * 160 + m8 * 16;
    ra = ((tok < 32 ? 5 : -2) + (tok >> 4)) * 20480 + (m8 >> 1) * 1024 + (m8 & 1) * 512 + (((tok & 15) ^ m8) << 4);
    xr = *(const u32x4*)(x + oo);
  };
  if (__builtin_expect(wid < 8, 1)) {
    if (threadIdx.x < 28) lds_st(smem + CNT + threadIdx.x * 4, 0u);
    float f[16];
#pragma unroll
    for (int i = 0; i < 4; ++i) { f[2 * i] = bf_lo(v0[i]); f[2 * i + 1] = bf_hi(v0[i]); f[8 + 2 * i] = bf_lo(v1[i]); f[9 + 2 * i] = bf_hi(v1[i]); }
    const float sh = rdl(f[0], 0);
    float s0 = 0.f, q0 = 0.f, s1 = 0.f, q1 = 0.f;
#pragma unroll
    for (int i = 0; i < 8; ++i) {
      f[i] -= sh; f[8 + i] -= sh;
      s0 += f[i]; q0 += f[i] * f[i];
      s1 += f[8 + i]; q1 += f[8 + i] * f[8 + i];
    }
    if (lane < 16) { s0 += s1; q0 += q1; }
    const float m = wave_sum(s0) * (1.f / 640.f);
    const float var = fmaxf(wave_sum(q0) * (1.f / 640.f) - m * m, 0.f);
    const float rs = rsqrtf(var + 1e-5f);
    const u64 tag = uni64(gen) + 1;
    if (lane < 2) *(float*)(smem + CSL + wid * 8 + lane * 4) = lane ? rs : m + sh;
    if (threadIdx.x == 0) *(u64*)(smem + C_TAG) = tag;
    asm volatile("s_waitcnt lgkmcnt(0)" :: "s"(tag) : "memory");
    if (ct < 18) g1_compute<0>(smem, rws, ws, rt, ct, wid, lane, tag);
    else if (ct < 22) g1_compute<1>(smem, rws, ws, rt, ct - 18, wid, lane, tag);
    else if (ct < 30) g1_compute<2>(smem, rws, ws, rt, ct - 22, wid, lane, tag);
    else {
      if (lane < 16) __builtin_amdgcn_raw_ptr_buffer_load_lds(rws, (LAS void*)(smem + DUMMY), 4, AF_OFF - 128 - ((sid * 8 + wid) + lane * 128) * 128, 0, 0, 16);
      spare_relayout(smem, rws, wd, ws, sid, wid, lane);
      {
        auto tl = [&](unsigned off) { __builtin_amdgcn_raw_ptr_buffer_load_lds(rws, (LAS void*)(smem + DUMMY), 4, off, 0, 0, 16); };
        const int u = sid * 8 + wid;
        lds_wait(smem + C_QK, 1);
        if (lane < 48) tl(KVP_OFF + (u * 48 + lane) * 128);
        if (lane < 50) tl(WF_OFF + ((u >> 4) * NS2 + 54) * 5120 + ((u & 15) * 50 + lane) * 128);
        drain();
      }
    }
    lane = lane_id();
    const int kq = wid & 3, th = wid >> 2;
    f32x4 acc[2][5];
#pragma unroll
    for (int i = 0; i < 2; ++i)
#pragma unroll
      for (int j = 0; j < 5; ++j) acc[i][j] = f32x4{0.f, 0.f, 0.f, 0.f};
    u32x4 af[3][2];
    bf16x8 wf[5];
    const unsigned aso = AF_OFF + r2 * NS2 * 4096 + th * 2048;
    auto lda = [&](int gI) {
      const unsigned so = aso + g2_step(gI, kq) * 4096;
      af[gI % 3][0] = ld16(rws, lane * 16, so);
      af[gI % 3][1] = ld16(rws, lane * 16 + 1024, so);
    };
    unsigned wl[3];
    auto rdw = [&](int gI, int j) { wf[j] = *(const LAS bf16x8*)(size_t)(wl[(gI % 7) / 3] + ((gI % 7) % 3) * 20480 + j * 1024); };
    const bool isq = ct >= 18 && ct < 22;
    bool pend = ct < 22 || (ct < 30 && wid < 5);
    auto announce = [&]() {
      if (ct < 18) {
        if (lane < 8) post(ws, (rt * 4 + kq) * 8 + lane, SL_H1 + th * 18 + ct, tag);
      } else if (arrive_last<63>(smem + C_M, isq ? 8 : 5) && lane < 32) {
        post(ws, isq ? rt * 32 + lane : lane * 8 + rt, isq ? SL_O + ct - 18 : SL_M + ct - 22, tag);
      }
    };
    if (pend && (ct < 18 || __builtin_amdgcn_readfirstlane(lds_ld_now(smem + C_H)) == 0)) { drain(); announce(); pend = false; }
    if (wid == 0 && ct < 18) { await<1>(box + SL_H * 8, 36, tag, lane); lds_st(smem + C_H, 1); }
    lds_wait(smem + C_H, 1);
#pragma unroll
    for (int gI = 0; gI < 3; ++gI) lda(gI);
    __builtin_amdgcn_sched_barrier(0);
    if (pend) { asm volatile("s_waitcnt vmcnt(%0)" :: "n"(2 * 3) : "memory"); announce(); }
#pragma unroll
    for (int k = 0; k < 3; ++k) { wl[k] = (unsigned)(size_t)(LAS char*)smem + kq * 5120 + lane * 16 + k * 61440; asm volatile("" : "+v"(wl[k])); }
    lds_wait(smem + C_FULL + kq * 4, 7);
#pragma unroll
    for (int j = 0; j < 5; ++j) rdw(0, j);
    __builtin_amdgcn_sched_barrier(0);
    unsigned full = 7;
    u64 oflag = 0;
#pragma unroll
    for (int gI = 0; gI < 19; ++gI) {
      if (gI + 1 >= 7 && gI + 1 < 19) {
        if ((unsigned)__builtin_amdgcn_readfirstlane(full) < (unsigned)(gI + 2)) lds_wait(smem + C_FULL + kq * 4, gI + 2);
      }
      if (gI == 13 && kq >= 2) { af[gI % 3][0] = u32x4{0u, 0u, 0u, 0u}; af[gI % 3][1] = u32x4{0u, 0u, 0u, 0u}; }
#pragma unroll
      for (int j = 0; j < 5; ++j) {
        acc[0][j] = mfma16(wf[j], __builtin_bit_cast(bf16x8, af[gI % 3][0]), acc[0][j]);
        acc[1][j] = mfma16(wf[j], __builtin_bit_cast(bf16x8, af[gI % 3][1]), acc[1][j]);
        __builtin_amdgcn_sched_barrier(0);
        if (gI + 1 < 19) rdw(gI + 1, j);
        if (j == 2 && gI + 2 >= 7 && gI + 2 < 19) full = lds_ld(smem + C_FULL + kq * 4);
        __builtin_amdgcn_sched_barrier(0);
      }
      if (gI + 1 < 12 || gI == 15) { asm volatile("" ::: "memory"); lds_st(smem + C_FREE + kq * 8 + th * 4, gI + 2); }
      if (gI == 14 - 3 - 3) oflag = __hip_atomic_load((const GAS u64*)(box + SL_O * 8) + kq, RLX_AGENT);
      if (gI == 14 - 3) {
        if (!__all(oflag == tag)) await<2>(box + (SL_O + kq) * 8, 1, tag, lane);
        __builtin_amdgcn_fence(__ATOMIC_ACQUIRE, "wavefront");
      }
      if (gI + 3 == 9) { lds_wait(smem + C_H1, 1); }
      if (gI + 3 < 19) lda(gI + 3);
      if (gI == 19 - 3) fin_prep();
      __builtin_amdgcn_sched_barrier(0);
    }
    lds_wait(smem + C_FREE + kq * 8 + (th ^ 1) * 4, 17);
    {
      lane = lane_id();
      const int t = lane & 15, g = lane >> 4;
#pragma unroll
      for (int i = 0; i < 2; ++i)
#pragma unroll
        for (int j = 0; j < 5; ++j)
          *(f32x4*)(smem + ((th ? 0 : 5) + i) * 20480 + kq * 5120 + j * 1024 + g * 256 + ((t ^ (j * 2 + (g >> 1))) << 4)) = acc[i][j];
    }
  } else {
    fin_prep();
  }
  asm volatile("s_waitcnt lgkmcnt(0)" ::: "memory");
  bar();
  if (wid < 10) {
    f32x4 lo = *(const f32x4*)(smem + ra), hi = *(const f32x4*)(smem + ra + 256);
#pragma unroll
    for (int k = 1; k < 4; ++k) { lo += *(const f32x4*)(smem + ra + k * 5120); hi += *(const f32x4*)(smem + ra + k * 5120 + 256); }
    const u32x4 ov = u32x4{pack2(lo[0] + bf_lo(xr[0]), lo[1] + bf_hi(xr[0])), pack2(lo[2] + bf_lo(xr[1]), lo[3] + bf_hi(xr[1])),
                                pack2(hi[0] + bf_lo(xr[2]), hi[1] + bf_hi(xr[2])), pack2(hi[2] + bf_lo(xr[3]), hi[3] + bf_hi(xr[3]))};
    *(u32x4*)(out + oo) = ov;
  }
}

extern "C" void kernel_launch(void* const* d_in, const int* in_sizes, int n_in, void* d_out, int out_size, void* d_ws,
                              size_t ws_size, hipStream_t stream) {
  (void)hipFuncSetAttribute((const void*)fused, hipFuncAttributeMaxDynamicSharedMemorySize, LDS_BYTES);
  fused<<<256, 768, LDS_BYTES, stream>>>((const char*)d_in[0], (const char*)d_in[1], (const char*)d_in[2], (const char*)d_in[4],
                                         (const char*)d_in[5], (const char*)d_in[6], (char*)d_out, (char*)d_ws);
}
```
